```python
import torch

def _softmax(x, axis=-1):
    return torch.softmax(x, dim=axis)
def _gelu(x):
    return torch.nn.functional.gelu(x, approximate="tanh")
def _silu(x):
    return torch.nn.functional.silu(x)
def _sigmoid(x):
    return torch.sigmoid(x)
def _layernorm(x, axis=-1, eps=1e-5):
    m = x.mean(dim=axis, keepdim=True)
    v = x.var(dim=axis, keepdim=True, unbiased=False)
    return (x - m) / torch.sqrt(v + eps)
def _scatter_add(tgt, idx, src, axis=0):
    return tgt.clone().index_add_(axis, idx, src)
def _segment_sum(src, seg, n):
    out = torch.zeros((n,) + src.shape[1:], dtype=src.dtype)
    return out.index_add_(0, seg, src)
def _pad(x, pw):
    flat = tuple(v for pair in reversed(pw) for v in pair)
    return torch.nn.functional.pad(x, flat)

kernel_name = 'recurrent_scan__compressed_kv_cla__geglu_ffn__prompt'

def setup_inputs():

    _w = (lambda a: (lambda t: t.to(torch.bfloat16) if t.is_floating_point() else t)(torch.as_tensor(a)))
    def _A_setup():
        B, H, Sq, Sk, D, M = 1, 32, 2048, 2048, 128, 512
        Dc = 4 * D
        return {
            "x": _w((lambda *s: torch.randn(*s, dtype=torch.bfloat16) * 0.05)(B, Sq, M)),
            "c_kv": _w((lambda *s: torch.randn(*s, dtype=torch.bfloat16) * 0.2)(B, Sk, Dc)),
            "w_q": _w((lambda *s: torch.randn(*s, dtype=torch.bfloat16) * 3.2)(M, H, D) * (M ** -0.5)),
            "w_uk": _w((lambda *s: torch.randn(*s, dtype=torch.bfloat16) * 3.2)(Dc, H, D) * (Dc ** -0.5)),
            "w_uv": _w((lambda *s: torch.randn(*s, dtype=torch.bfloat16) * 0.2)(Dc, H, D) * (Dc ** -0.5)),
            "w_o": _w((lambda *s: torch.randn(*s, dtype=torch.bfloat16) * 0.2)(H, D, M) * ((H * D) ** -0.5)),
        }
    _A_in = _A_setup()

    def _B_setup():
        B, S, D, F = 1, 2048, 512, 1376
        return {
            "x": _w((lambda *s: torch.randn(*s, dtype=torch.bfloat16) * 0.05)(B, S, D)),
            "w_gate": _w((lambda *s: torch.randn(*s, dtype=torch.bfloat16) * 0.1)(D, F)),
            "w_up": _w((lambda *s: torch.randn(*s, dtype=torch.bfloat16) * 0.1)(D, F)),
            "w_down": _w((lambda *s: torch.randn(*s, dtype=torch.bfloat16) * 0.1)(F, D)),
        }
    _B_in = _B_setup()

    return {
        "A_x": _A_in["x"],
        "A_c_kv": _A_in["c_kv"],
        "A_w_q": _A_in["w_q"],
        "A_w_uk": _A_in["w_uk"],
        "A_w_uv": _A_in["w_uv"],
        "A_w_o": _A_in["w_o"],
        "B_x": _B_in["x"],
        "B_w_gate": _B_in["w_gate"],
        "B_w_up": _B_in["w_up"],
        "B_w_down": _B_in["w_down"],
    }

def reference(A_x, A_c_kv, A_w_q, A_w_uk, A_w_uv, A_w_o, B_x, B_w_gate, B_w_up, B_w_down):
    def _A_ref(x, c_kv, w_q, w_uk, w_uv, w_o):
        B, Sq, M = x.shape[0], x.shape[1], x.shape[2]
        Sk = c_kv.shape[1]
        H, D = w_q.shape[1], w_q.shape[2]
        q = torch.einsum("bqm,mhd->bhqd", x, w_q)
        k = torch.einsum("bkc,chd->bhkd", c_kv, w_uk)
        v = torch.einsum("bkc,chd->bhkd", c_kv, w_uv)
        scores = torch.einsum("bhqd,bhkd->bhqk", q, k) * (D ** -0.5)
        off = Sk - Sq
        qi = torch.arange(Sq)[:, None]
        kj = torch.arange(Sk)[None, :]
        cmask = kj <= (qi + off)
        scores = torch.where(cmask[None, None], scores, -1e9)
        p = _softmax(scores, axis=-1)
        ctx = torch.einsum("bhqk,bhkd->bhqd", p, v)
        return torch.einsum("bhqd,hdm->bqm", ctx, w_o)

    def _B_ref(x, w_gate, w_up, w_down):
        return (_gelu(x @ w_gate) * (x @ w_up)) @ w_down

    x = A_x
    xn = _layernorm(x, axis=-1)
    s = _A_ref(xn, A_c_kv, A_w_q, A_w_uk, A_w_uv, A_w_o)
    g = _silu(_B_ref(xn, B_w_gate, B_w_up, B_w_down))
    return x + s * g
```

```cpp
#include <hip/hip_runtime.h>
#include <stdint.h>

typedef short bf16x8 __attribute__((ext_vector_type(8)));
typedef short s16x4 __attribute__((ext_vector_type(4)));
typedef float f32x16 __attribute__((ext_vector_type(16)));
typedef float f32x4 __attribute__((ext_vector_type(4)));
typedef float f32x2 __attribute__((ext_vector_type(2)));
typedef __bf16 bf16x2_t __attribute__((ext_vector_type(2)));
typedef unsigned u32x4 __attribute__((ext_vector_type(4)));
typedef unsigned u32x2 __attribute__((ext_vector_type(2)));
typedef unsigned short u16;
#define LAS __attribute__((address_space(3)))
typedef LAS const char* lds_cptr;
typedef LAS char* lds_ptr;
#define DI __device__ __forceinline__
#define SBAR() __builtin_amdgcn_sched_barrier(0)
#define PIN(x) asm volatile("" : "+v"(x))
#define MFMA(a, b, c) __builtin_amdgcn_mfma_f32_32x32x16_bf16((a), (b), (c), 0, 0, 0)

#define STAMP(kern, idx) do {} while (0)
#define TSARG
#define TSPASS
constexpr int SEQ = 2048, DM = 512, NH = 32, HD = 128, FF = 1376;
constexpr int NFB = FF / 32;
constexpr int NKS_H = FF / 16;
constexpr size_t MiB = 1u << 20;
constexpr size_t WS_XN = 0, WS_K = 2 * MiB, WS_V = 18 * MiB, WS_CTX = 34 * MiB, WS_H = 50 * MiB;

DI unsigned rfl(unsigned x) { return __builtin_amdgcn_readfirstlane(x); }
DI unsigned cvtpk(float lo, float hi) { f32x2 v = {lo, hi}; bf16x2_t b = __builtin_convertvector(v, bf16x2_t); return __builtin_bit_cast(unsigned, b); }
DI float bf2f(u16 b) { return __uint_as_float((unsigned)b << 16); }
DI bf16x8 lds_rd128(lds_cptr p) { return *(const LAS bf16x8*)p; }
typedef short v4i16_t __attribute__((ext_vector_type(4)));
DI s16x4 lds_tr(lds_cptr p) { return __builtin_bit_cast(s16x4, __builtin_amdgcn_ds_read_tr16_b64_v4i16((LAS v4i16_t*)p)); }
DI bf16x8 cat8(s16x4 a, s16x4 b) { return __builtin_shufflevector(a, b, 0, 1, 2, 3, 4, 5, 6, 7); }
DI __amdgpu_buffer_rsrc_t mk_srd(const void* p, unsigned bytes) { return __builtin_amdgcn_make_buffer_rsrc((void*)p, (short)0, (int)bytes, 0x00020000); }
DI void dma16(unsigned m0v, unsigned voff, __amdgpu_buffer_rsrc_t srd, unsigned soff) {
    asm volatile("s_mov_b32 m0, %0\n\ts_nop 0\n\tbuffer_load_dwordx4 %1, %2, %3 offen lds" ::"s"(m0v), "v"(voff), "s"(srd), "s"(soff) : "memory");
}
DI void dma16x4(unsigned m0v, unsigned voff, __amdgpu_buffer_rsrc_t srd, unsigned soff) {
    asm volatile("s_mov_b32 m0, %0\n\ts_nop 0\n\tbuffer_load_dwordx4 %1, %2, %3 offen lds\n\tbuffer_load_dwordx4 %1, %2, %3 offen offset:1024 lds\n\t"
                 "buffer_load_dwordx4 %1, %2, %3 offen offset:2048 lds\n\tbuffer_load_dwordx4 %1, %2, %3 offen offset:3072 lds"
                 ::"s"(m0v), "v"(voff), "s"(srd), "s"(soff) : "memory");
}
DI void dma16x2(unsigned m0v, unsigned voff, __amdgpu_buffer_rsrc_t srd, unsigned soff) {
    asm volatile("s_mov_b32 m0, %0\n\ts_nop 0\n\tbuffer_load_dwordx4 %1, %2, %3 offen lds\n\tbuffer_load_dwordx4 %1, %2, %3 offen offset:1024 lds"
                 ::"s"(m0v), "v"(voff), "s"(srd), "s"(soff) : "memory");
}
#define WAITV(n) asm volatile("s_waitcnt vmcnt(" #n ")" ::: "memory")
#define BARRIER() asm volatile("s_barrier" ::: "memory")
DI u32x4 pack8(const f32x16& x, int s, float sc) {
    u32x4 w;
    w[0] = cvtpk(x[8 * s + 0] * sc, x[8 * s + 1] * sc); w[1] = cvtpk(x[8 * s + 2] * sc, x[8 * s + 3] * sc);
    w[2] = cvtpk(x[8 * s + 4] * sc, x[8 * s + 5] * sc); w[3] = cvtpk(x[8 * s + 6] * sc, x[8 * s + 7] * sc);
    return w;
}
DI u32x4 pack8n(const f32x16& x, int s) {
    u32x4 w;
    w[0] = cvtpk(x[8 * s + 0], x[8 * s + 1]); w[1] = cvtpk(x[8 * s + 2], x[8 * s + 3]);
    w[2] = cvtpk(x[8 * s + 4], x[8 * s + 5]); w[3] = cvtpk(x[8 * s + 6], x[8 * s + 7]);
    return w;
}

__global__ __launch_bounds__(512) void k_ln(const u16* __restrict__ x, u16* __restrict__ xnF) {
    const int lane = threadIdx.x & 63, wid = threadIdx.x >> 6;
    const int row = blockIdx.x * 8 + wid;
    const u32x4 raw = *(const u32x4*)(x + (size_t)row * DM + lane * 8);
    float v[8];
#pragma unroll
    for (int i = 0; i < 4; ++i) { v[2 * i] = __uint_as_float(raw[i] << 16); v[2 * i + 1] = __uint_as_float(raw[i] & 0xffff0000u); }
    float s = 0.f;
#pragma unroll
    for (int i = 0; i < 8; ++i) s += v[i];
#pragma unroll
    for (int o = 1; o < 64; o <<= 1) s += __shfl_xor(s, o);
    const float mean = s * (1.f / DM);
    float q = 0.f;
#pragma unroll
    for (int i = 0; i < 8; ++i) { v[i] -= mean; q += v[i] * v[i]; }
#pragma unroll
    for (int o = 1; o < 64; o <<= 1) q += __shfl_xor(q, o);
    const float rstd = 1.f / sqrtf(q * (1.f / DM) + 1e-5f);
    u32x4 w;
#pragma unroll
    for (int i = 0; i < 4; ++i) w[i] = cvtpk(v[2 * i] * rstd, v[2 * i + 1] * rstd);
    *(u32x4*)((char*)xnF + ((size_t)((row >> 5) * 32 + (lane >> 1)) * 64 + (row & 31) + 32 * (lane & 1)) * 16) = w;
}

template <bool isV>
DI void kv_tile(lds_ptr lds, unsigned lds0, int lane, int wid, int h, int kb8, const u16* __restrict__ ckv, const u16* __restrict__ wsel, u16* __restrict__ OF) {
    const int r32 = lane & 31, hi = lane >> 5, wr = wid & 1, wc1 = (wid >> 1) & 1;
    const int trq = (lane & 15) >> 2, trp = lane & 3, trb = (lane >> 4) & 1;
    const __amdgpu_buffer_rsrc_t srdA = mk_srd(ckv + (size_t)kb8 * 256 * DM, 256 * DM * 2);
    const __amdgpu_buffer_rsrc_t srdB = mk_srd(wsel + h * HD, DM * 4096 * 2 - h * HD * 2);
    unsigned voA[2];
#pragma unroll
    for (int par = 0; par < 2; ++par) voA[par] = (unsigned)((32 * wid + (lane >> 3)) * 1024 + (((lane & 7) ^ (4 * par + (lane >> 4))) * 16));
    const unsigned voB = (unsigned)((((lane >> 2) & 7) + (wid & 3) * 16) * 8192 + (lane >> 5) * 64 + (lane & 3) * 16);
    const unsigned m0A = lds0 + wid * 4096, m0B = lds0 + 32768 + wid * 4096;
    auto stage = [&](int kt, int slot) {
#pragma unroll
        for (int i = 0; i < 4; ++i) dma16(m0A + slot * 65536 + i * 1024, voA[i & 1], srdA, (unsigned)(kt * 128 + i * 8192));
#pragma unroll
        for (int i = 0; i < 4; ++i) dma16(m0B + slot * 65536 + i * 1024, voB, srdB, (unsigned)(kt * 64 * 8192 + (i >> 1) * 8 * 8192 + (i & 1) * 128));
    };
    unsigned aoff[4];
#pragma unroll
    for (int ks = 0; ks < 4; ++ks) aoff[ks] = (unsigned)((wr * 128 + r32) * 128 + (((2 * ks + hi) ^ ((r32 >> 1) & 7)) * 16));
    const unsigned boff = (unsigned)(32768 + (isV ? 16384 : 0) + wc1 * 1024 + hi * 2048 + trq * 64 + trb * 32 + trp * 8);
    f32x16 acc[4][2];
#pragma unroll
    for (int a = 0; a < 4; ++a)
#pragma unroll
        for (int b = 0; b < 2; ++b) acc[a][b] = f32x16{};
    bf16x8 cf[2][4], wf[2][2];
    auto rdfrags = [&](int buf, int slot, int ks) {
#pragma unroll
        for (int db = 0; db < 2; ++db) {
            const lds_cptr p = lds + slot * 65536 + boff + ks * 4096 + db * 512;
            wf[buf][db] = cat8(lds_tr(p), lds_tr(p + 256));
        }
#pragma unroll
        for (int kb = 0; kb < 4; ++kb) cf[buf][kb] = lds_rd128(lds + slot * 65536 + aoff[ks] + kb * 4096);
    };
    asm volatile("s_nop 4");
    stage(0, 0); stage(1, 1);
    WAITV(8); BARRIER();
    rdfrags(0, 0, 0);
#define KV_MMA(buf) do { _Pragma("unroll") for (int kb = 0; kb < 4; ++kb) _Pragma("unroll") for (int db = 0; db < 2; ++db) \
        acc[kb][db] = isV ? MFMA(cf[buf][kb], wf[buf][db], acc[kb][db]) : MFMA(wf[buf][db], cf[buf][kb], acc[kb][db]); } while (0)
#define KV_ITER(kt, slot) do { \
    rdfrags(1, slot, 1); KV_MMA(0); \
    rdfrags(0, slot, 2); KV_MMA(1); \
    rdfrags(1, slot, 3); KV_MMA(0); \
    asm volatile("s_waitcnt vmcnt(0) lgkmcnt(0)\n\ts_barrier" ::: "memory"); \
    if ((kt) + 2 < 8) stage((kt) + 2, slot); \
    if ((kt) + 1 < 8) rdfrags(0, (slot) ^ 1, 0); \
    KV_MMA(1); } while (0)
    for (int kt = 0; kt < 8; kt += 2) { KV_ITER(kt, 0); KV_ITER(kt + 1, 1); }
#undef KV_ITER
#undef KV_MMA
    char* base = (char*)OF + (size_t)h * 64 * 8192 + lane * 16;
#pragma unroll
    for (int kb = 0; kb < 4; ++kb) {
        const int u = kb8 * 8 + wr * 4 + kb;
#pragma unroll
        for (int db = 0; db < 2; ++db) {
            const int dblk = 2 * wc1 + db;
#pragma unroll
            for (int s = 0; s < 2; ++s) {
                const int fi = isV ? (s * 4 + dblk) : (2 * dblk + s);
                *(u32x4*)(base + (size_t)(u * 8 + fi) * 1024) = pack8n(acc[kb][db], s);
            }
        }
    }
}
__global__ __launch_bounds__(512, 2) void k_proj(const u16* __restrict__ ckv, const u16* __restrict__ wuk, const u16* __restrict__ wuv,
                                                 const u16* __restrict__ xnF, const u16* __restrict__ wg, const u16* __restrict__ wu,
                                                 u16* __restrict__ KF, u16* __restrict__ VF, u16* __restrict__ HF TSARG) {
    STAMP(1, 0);
    extern __shared__ __attribute__((aligned(16))) char lds_raw[];
    const lds_ptr lds = (lds_ptr)lds_raw;
    const unsigned lds0 = (unsigned)(uintptr_t)lds_raw;
    const int tid = threadIdx.x, lane = tid & 63, r32 = lane & 31, hi = lane >> 5;
    const int wid = rfl(tid >> 6), wr = wid >> 2, wc = wid & 3;
    const int bx = blockIdx.x, xcd = bx & 7, bi = bx >> 3;
    const int trq = (lane & 15) >> 2, trp = lane & 3, trb = (lane >> 4) & 1;
    {
        const int h = xcd + 8 * (bi >> 3), kb8 = bi & 7;
        if (wid >= 4) kv_tile<true>(lds, lds0, lane, wid, h, kb8, ckv, wuv, VF);
        else kv_tile<false>(lds, lds0, lane, wid, h, kb8, ckv, wuk, KF);
    }
    STAMP(1, 1);
    if (bx < 176) {
        const int rt = (bx & 7) * 2 + ((bx >> 3) & 1), ft = bx >> 4;
        const __amdgpu_buffer_rsrc_t srdA = mk_srd(xnF + (size_t)rt * 4 * 32 * 512, 4 * 32 * 1024);
        const u16* wsel = (wid >> 2) ? wu : wg;
        const __amdgpu_buffer_rsrc_t srdB = mk_srd(wsel + ft * 128, DM * FF * 2 - ft * 256);
        const unsigned voA = (unsigned)(lane * 16 + (wid >> 1) * 32768 + (wid & 1) * 2048);
        const unsigned voB = (unsigned)((((lane >> 2) & 7) + (wid & 3) * 16) * (FF * 2) + (lane >> 5) * 64 + (lane & 3) * 16);
        const unsigned m0A = lds0 + wid * 2048, m0B = lds0 + 32768 + wid * 4096;
        auto stage = [&](int kt, int slot) {
            dma16x2(m0A + slot * 65536, voA, srdA, (unsigned)(kt * 4096));
#pragma unroll
            for (int i = 0; i < 4; ++i) dma16(m0B + slot * 65536 + i * 1024, voB, srdB, (unsigned)((kt * 64 + (i >> 1) * 8) * (FF * 2) + (i & 1) * 128));
        };
        const unsigned aoff = (unsigned)(wr * 2 * 4096 + lane * 16);
        const unsigned boff = (unsigned)(32768 + wc * 512 + hi * 2048 + trq * 64 + trb * 32 + trp * 8);
        f32x16 ag[2], au[2];
        ag[0] = f32x16{}; ag[1] = f32x16{}; au[0] = f32x16{}; au[1] = f32x16{};
        bf16x8 xf[2][2], wf[2][2];
        auto rdfrags = [&](int buf, int slot, int ks) {
#pragma unroll
            for (int pt = 0; pt < 2; ++pt) {
                const lds_cptr p = lds + slot * 65536 + boff + ks * 4096 + pt * 16384;
                wf[buf][pt] = cat8(lds_tr(p), lds_tr(p + 256));
            }
#pragma unroll
            for (int q = 0; q < 2; ++q) xf[buf][q] = lds_rd128(lds + slot * 65536 + aoff + q * 4096 + ks * 1024);
        };
        asm volatile("s_nop 4");
        stage(0, 0); stage(1, 1);
        WAITV(6); BARRIER();
        rdfrags(0, 0, 0);
#define FF_MMA(buf) do { _Pragma("unroll") for (int q = 0; q < 2; ++q) { ag[q] = MFMA(wf[buf][0], xf[buf][q], ag[q]); au[q] = MFMA(wf[buf][1], xf[buf][q], au[q]); } } while (0)
#define FF_ITER(kt, slot) do { \
        rdfrags(1, slot, 1); FF_MMA(0); \
        rdfrags(0, slot, 2); FF_MMA(1); \
        rdfrags(1, slot, 3); FF_MMA(0); \
        asm volatile("s_waitcnt vmcnt(0) lgkmcnt(0)\n\ts_barrier" ::: "memory"); \
        if ((kt) + 2 < 8) stage((kt) + 2, slot); \
        if ((kt) + 1 < 8) rdfrags(0, (slot) ^ 1, 0); \
        FF_MMA(1); } while (0)
        for (int kt = 0; kt < 8; kt += 2) { FF_ITER(kt, 0); FF_ITER(kt + 1, 1); }
#undef FF_ITER
#undef FF_MMA
        const int fb = ft * 4 + wc;
        if (fb < NFB) {
#pragma unroll
            for (int q = 0; q < 2; ++q) {
                f32x16 hh;
#pragma unroll
                for (int r = 0; r < 16; ++r) {
                    const float g = ag[q][r];
                    const float z = g * (1.5957691216f + 0.0713548163f * g * g);
                    const float e = __builtin_amdgcn_exp2f(-1.4426950409f * z);
                    hh[r] = g * __builtin_amdgcn_rcpf(1.f + e) * au[q][r];
                }
                const int qg = rt * 4 + wr * 2 + q;
#pragma unroll
                for (int s = 0; s < 2; ++s)
                    *(u32x4*)((char*)HF + ((size_t)(qg * NKS_H + fb * 2 + s) * 64 + lane) * 16) = pack8n(hh, s);
            }
        }
    }
    STAMP(1, 2);
}

constexpr int L_K = 0, L_V = 65536, L_X = 131072, LDS_ATT = 163840;
constexpr float QSCALE = 0.08838834764831845f * 1.4426950408889634f;
constexpr float THR = 8.f;
#define MX3(a, b, c) __builtin_fmaxf(__builtin_fmaxf((a), (b)), (c))
#define EX(v) __builtin_amdgcn_exp2f(v)

struct AttSt {
    f32x16 o[4]; bf16x8 qf[8]; bf16x8 fr[8]; f32x16 negm;
    float l, m, fp; bool pend;
};
DI void cmask(f32x16& p, int dq) {
#pragma unroll
    for (int r = 0; r < 16; ++r) { const int c = (r & 3) + 8 * (r >> 2); if (c > dq) p[r] = -__builtin_inff(); }
}
template <bool DMA>
DI void half_step(AttSt& s, f32x16& Sn, f32x16& Sc, u32x4 (&Pc)[2], u32x4 (&Pp)[2], lds_cptr kp, lds_cptr vp, lds_cptr knp,
                  bool domask, int dq, bool docheck,
                  unsigned dm0, unsigned dvo, __amdgpu_buffer_rsrc_t dsrd, unsigned dso) {
    float r0, r1, ma, mb;
#define RD(i) do { constexpr int n_ = (i) + 6; s.fr[n_ & 7] = lds_rd128((n_ < 8 ? kp : n_ < 16 ? vp : knp) + (n_ & 7) * 1024); } while (0)
#define QK(i) Sn = MFMA(s.fr[i], s.qf[i], (i) == 0 ? s.negm : Sn)
#define PV(i) s.o[(i) & 3] = MFMA(s.fr[i], __builtin_bit_cast(bf16x8, Pp[(i) >> 2]), s.o[(i) & 3])
#define E(i) Sc[i] = EX(Sc[i])
#define C(i) Pc[(i) >> 2][(i) & 3] = cvtpk(Sc[2 * (i)], Sc[2 * (i) + 1])
#define ENDG() do { PIN(Sc); SBAR(); } while (0)
    SBAR();
    QK(0); RD(0); E(0); E(1); ENDG();
    QK(1); RD(1); E(2); E(3); C(0); PIN(Pc[0]); ENDG();
    QK(2); RD(2); E(4); E(5); r0 = Sc[0] + Sc[2]; PIN(r0); ENDG();
    QK(3); RD(3); E(6); E(7); r1 = Sc[1] + Sc[3]; PIN(r1); ENDG();
    QK(4); RD(4); E(8); C(1); r0 += Sc[4]; r1 += Sc[5]; PIN(r0); PIN(r1); PIN(Pc[0]); ENDG();
    QK(5); RD(5); E(9); C(2); r0 += Sc[6]; r1 += Sc[7]; PIN(r0); PIN(r1); PIN(Pc[0]); ENDG();
    QK(6); RD(6); E(10); C(3); r0 += Sc[8]; PIN(r0); PIN(Pc[0]); ENDG();
    QK(7); RD(7); E(11); r1 += Sc[9]; C(4); PIN(r1); PIN(Pc[1]); ENDG();
    PV(0); RD(8); E(12); r0 += Sc[10]; PIN(r0); ENDG();
    if constexpr (DMA) { dma16x4(dm0, dvo, dsrd, dso); SBAR(); }
    PV(1); RD(9); E(13); r1 += Sc[11]; C(5); PIN(r1); PIN(Pc[1]); ENDG();
    if (domask) cmask(Sn, dq);
    SBAR();
    PV(2); RD(10); E(14); r0 += Sc[12]; ma = MX3(Sn[0], Sn[1], Sn[2]); mb = MX3(Sn[3], Sn[4], Sn[5]); PIN(r0); PIN(ma); PIN(mb); ENDG();
    PV(3); RD(11); E(15); r1 += Sc[13]; C(6); ma = MX3(ma, Sn[6], Sn[7]); PIN(r1); PIN(Pc[1]); PIN(ma); ENDG();
    PV(4); RD(12); r0 += Sc[14]; mb = MX3(mb, Sn[8], Sn[9]); ma = MX3(ma, Sn[10], Sn[11]); mb = MX3(mb, Sn[12], Sn[13]); PIN(r0); PIN(ma); PIN(mb); ENDG();
    PV(5); RD(13); r1 += Sc[15]; C(7); ma = MX3(ma, Sn[14], Sn[15]); PIN(r1); PIN(Pc[1]); PIN(ma); ENDG();
    PV(6); RD(14); ma = __builtin_fmaxf(ma, mb); r0 += r1; s.l += r0; PIN(ma); PIN(s.l); SBAR();
    PV(7); RD(15); SBAR();
    const bool need = docheck && __any(ma > THR);
    if (__builtin_expect(need || s.pend, 0)) {
        if (s.pend) {
#pragma unroll
            for (int d = 0; d < 4; ++d)
#pragma unroll
                for (int r = 0; r < 16; ++r) s.o[d][r] *= s.fp;
            s.pend = false;
        }
        if (need) {
            auto rr = __builtin_amdgcn_permlane32_swap(__float_as_uint(ma), __float_as_uint(ma), false, false);
            const float rm = __builtin_fmaxf(__uint_as_float(rr[0]), __uint_as_float(rr[1]));
            const float dl = __builtin_fmaxf(rm, 0.f);
            s.m += dl;
#pragma unroll
            for (int r = 0; r < 16; ++r) { Sn[r] -= dl; s.negm[r] = -s.m; }
            s.fp = EX(-dl); s.l *= s.fp; s.pend = true;
        }
    }
    SBAR();
#undef RD
#undef QK
#undef PV
#undef E
#undef C
#undef ENDG
}

template <bool LIGHT>
DI void att_main(AttSt& s, lds_ptr lds, unsigned lds0, int lane, int wq, int qg, int NT,
                 __amdgpu_buffer_rsrc_t srdK, __amdgpu_buffer_rsrc_t srdV, u16* __restrict__ ctxF, int h TSARG) {
    STAMP(2, 1);
    const int r32 = lane & 31, hi = lane >> 5;
    const int tl = qg >> 1;
    const int row = qg * 32 + r32;
    const lds_cptr kb = lds + L_K + lane * 16, vb = lds + L_V + lane * 16;
    const unsigned dvo = (unsigned)(lane * 16 + wq * 4096);
    f32x16 Sa, Sb; u32x4 Pa[2], Pb[2];
    Pb[0] = u32x4{0, 0, 0, 0}; Pb[1] = u32x4{0, 0, 0, 0}; Pa[0] = Pb[0]; Pa[1] = Pb[0];
    {
        f32x16 z = f32x16{};
#pragma unroll
        for (int i = 0; i < 8; ++i) { const bf16x8 f = lds_rd128(kb + i * 1024); z = MFMA(f, s.qf[i], z); }
        Sa = z;
        if (tl == 0) cmask(Sa, row - 4 * hi);
        float ma = MX3(Sa[0], Sa[1], Sa[2]);
#pragma unroll
        for (int r = 3; r < 15; r += 2) ma = MX3(ma, Sa[r], Sa[r + 1]);
        ma = __builtin_fmaxf(ma, Sa[15]);
        auto rr = __builtin_amdgcn_permlane32_swap(__float_as_uint(ma), __float_as_uint(ma), false, false);
        const float rm = __builtin_fmaxf(__uint_as_float(rr[0]), __uint_as_float(rr[1]));
        s.m = rm; s.l = 0.f; s.fp = 1.f; s.pend = false;
#pragma unroll
        for (int r = 0; r < 16; ++r) { Sa[r] -= rm; s.negm[r] = -rm; }
#pragma unroll
        for (int d = 0; d < 4; ++d) s.o[d] = f32x16{};
#pragma unroll
        for (int i = 0; i < 6; ++i) s.fr[i] = lds_rd128(kb + 8192 + i * 1024);
    }
    int t = 0;
    for (; t <= tl; ++t) {
        const int sk = (t & 3) * 16384, sk1 = ((t + 1) & 3) * 16384, svp = (t == 0 ? 0 : ((t - 1) & 3) * 16384 + 8192);
        const bool last = (t == tl);
        half_step<LIGHT>(s, Sb, Sa, Pa, Pb, kb + sk + 8192, vb + svp, kb + sk1, last, row - (64 * t + 32) - 4 * hi, true,
                         rfl(lds0 + L_K + ((t + 3) & 3) * 16384 + wq * 4096), dvo, srdK, (unsigned)(t + 3) * 16384u);
        half_step<LIGHT>(s, Sa, Sb, Pb, Pa, kb + sk1, vb + sk, kb + sk1 + 8192, t + 1 == tl, row - (64 * t + 64) - 4 * hi, !last,
                         rfl(lds0 + L_V + ((t + 2) & 3) * 16384 + wq * 4096), dvo, srdV, (unsigned)(t + 2) * 16384u);
        if (last) {
#pragma unroll
            for (int i = 0; i < 8; ++i) { const bf16x8 f = lds_rd128(vb + sk + 8192 + i * 1024); s.o[i & 3] = MFMA(f, __builtin_bit_cast(bf16x8, Pb[i >> 2]), s.o[i & 3]); }
        }
        if constexpr (LIGHT) WAITV(8);
        BARRIER();
    }
    STAMP(2, 2);
    {
        auto rr = __builtin_amdgcn_permlane32_swap(__float_as_uint(s.l), __float_as_uint(s.l), false, false);
        const float lt = __uint_as_float(rr[0]) + __uint_as_float(rr[1]);
        const float inv = __builtin_amdgcn_rcpf(lt);
        char* base = (char*)ctxF + ((size_t)(qg * 256 + h * 8) * 64 + lane) * 16;
#pragma unroll
        for (int d = 0; d < 4; ++d)
#pragma unroll
            for (int q = 0; q < 2; ++q) *(u32x4*)(base + (d * 2 + q) * 1024) = pack8(s.o[d], q, inv);
    }
    for (; t < NT; ++t) {
        if constexpr (LIGHT) {
            dma16x4(rfl(lds0 + L_K + ((t + 3) & 3) * 16384 + wq * 4096), dvo, srdK, (unsigned)(t + 3) * 16384u);
            dma16x4(rfl(lds0 + L_V + ((t + 2) & 3) * 16384 + wq * 4096), dvo, srdV, (unsigned)(t + 2) * 16384u);
            WAITV(8);
        }
        BARRIER();
    }
}

__global__ __launch_bounds__(512, 2) void k_attn(const u16* __restrict__ xnF, const u16* __restrict__ wq, const u16* __restrict__ KF,
                                                 const u16* __restrict__ VF, u16* __restrict__ ctxF TSARG) {
    STAMP(2, 0);
    extern __shared__ __attribute__((aligned(16))) char lds_raw[];
    const lds_ptr lds = (lds_ptr)lds_raw;
    const unsigned lds0 = (unsigned)(uintptr_t)lds_raw;
    const int tid = threadIdx.x, lane = tid & 63, hi = lane >> 5;
    const int wid = rfl(tid >> 6), w4 = wid & 3;
    const int bx = blockIdx.x, xcd = bx & 7, bi = bx >> 3;
    const int h = xcd + 8 * (bi >> 3), j = bi & 7;
    const bool light = wid >= 4;
    const int qb = light ? j : 15 - j, qg = qb * 4 + w4;
    const int NT = 2 * (15 - j) + 2;
    const unsigned kv_bytes = (unsigned)NT * 16384u;
    const __amdgpu_buffer_rsrc_t srdK = mk_srd((const char*)KF + (size_t)h * 64 * 8192, kv_bytes);
    const __amdgpu_buffer_rsrc_t srdV = mk_srd((const char*)VF + (size_t)h * 64 * 8192, kv_bytes);
    AttSt s;
    {
        const __amdgpu_buffer_rsrc_t srdW = mk_srd(wq + h * HD, DM * 4096 * 2 - h * HD * 2);
        const __amdgpu_buffer_rsrc_t srdX = mk_srd((const char*)xnF + (size_t)qg * 32768, 32768);
        const unsigned voW = (unsigned)((((lane >> 2) & 7) + wid * 16) * 8192 + (lane >> 5) * 64 + (lane & 3) * 16);
        const unsigned vo16 = (unsigned)(lane * 16);
        constexpr int cbase[4] = {L_V, L_K + 32768, L_V + 32768, L_X};
        bf16x8 xf[32];
        asm volatile("s_nop 4");
#pragma unroll
        for (int c = 0; c < 4; ++c) {
#pragma unroll
            for (int i = 0; i < 4; ++i)
                dma16(lds0 + cbase[c] + wid * 4096 + i * 1024, voW, srdW, (unsigned)((c * 128 + (i >> 1) * 8) * 8192 + (i & 1) * 128));
#pragma unroll
            for (int i = 0; i < 8; ++i)
                asm volatile("buffer_load_dwordx4 %0, %1, %2, %3 offen offset:%4" : "=v"(xf[c * 8 + i]) : "v"(vo16), "s"(srdX), "s"((unsigned)(c * 8192 + (i >> 2) * 4096)), "i"((i & 3) * 1024) : "memory");
        }
        dma16x4(lds0 + L_K + wid * 4096, vo16, srdK, (unsigned)(wid * 4096));
        const int trq = (lane & 15) >> 2, trp = lane & 3, trb = (lane >> 4) & 1;
        const unsigned boff = (unsigned)(hi * 2048 + trq * 64 + trb * 32 + trp * 8);
        f32x16 qa[4];
#pragma unroll
        for (int d = 0; d < 4; ++d) qa[d] = f32x16{};
#define XW(c) "+v"(xf[(c) * 8 + 0]), "+v"(xf[(c) * 8 + 1]), "+v"(xf[(c) * 8 + 2]), "+v"(xf[(c) * 8 + 3]), "+v"(xf[(c) * 8 + 4]), "+v"(xf[(c) * 8 + 5]), "+v"(xf[(c) * 8 + 6]), "+v"(xf[(c) * 8 + 7])
#define QCHUNK(c) do { _Pragma("unroll") for (int k = 0; k < 8; ++k) { _Pragma("unroll") for (int d = 0; d < 4; ++d) { \
            const lds_cptr p = lds + cbase[c] + boff + k * 4096 + d * 512; \
            const bf16x8 wfr = cat8(lds_tr(p), lds_tr(p + 256)); \
            qa[d] = MFMA(wfr, xf[(c) * 8 + k], qa[d]); } } } while (0)
        asm volatile("s_waitcnt vmcnt(40)\n\ts_barrier" : XW(0)::"memory");
        QCHUNK(0);
        asm volatile("s_waitcnt vmcnt(28) lgkmcnt(0)\n\ts_barrier" : XW(1)::"memory");
        dma16x4(lds0 + L_V + wid * 4096, vo16, srdV, (unsigned)(wid * 4096));
        QCHUNK(1);
        asm volatile("s_waitcnt vmcnt(20) lgkmcnt(0)\n\ts_barrier" : XW(2)::"memory");
        dma16x2(lds0 + L_K + 32768 + wid * 2048, vo16, srdK, (unsigned)(32768 + wid * 2048));
        QCHUNK(2);
        asm volatile("s_waitcnt vmcnt(10) lgkmcnt(0)\n\ts_barrier" : XW(3)::"memory");
        QCHUNK(3);
#undef XW
#undef QCHUNK
#pragma unroll
        for (int d = 0; d < 4; ++d)
#pragma unroll
            for (int q = 0; q < 2; ++q) s.qf[2 * d + q] = __builtin_bit_cast(bf16x8, pack8(qa[d], q, QSCALE));
        asm volatile("s_waitcnt vmcnt(0) lgkmcnt(0)\n\ts_barrier" ::: "memory");
    }
    if (light) att_main<true>(s, lds, lds0, lane, w4, qg, NT, srdK, srdV, ctxF, h TSPASS);
    else att_main<false>(s, lds, lds0, lane, w4, qg, NT, srdK, srdV, ctxF, h TSPASS);
    WAITV(0);
    STAMP(2, 3);
}

__global__ __launch_bounds__(512, 2) void k_out(const u16* __restrict__ ctxF, const u16* __restrict__ HF, const u16* __restrict__ wo,
                                                const u16* __restrict__ wd, const u16* __restrict__ x, u16* __restrict__ out TSARG) {
    STAMP(3, 0);
    extern __shared__ __attribute__((aligned(16))) char lds_raw[];
    const lds_ptr lds = (lds_ptr)lds_raw;
    const unsigned lds0 = (unsigned)(uintptr_t)lds_raw;
    const int tid = threadIdx.x, lane = tid & 63, r32 = lane & 31, hi = lane >> 5;
    const int wid = rfl(tid >> 6);
    const int bx = blockIdx.x, xcd = bx & 7, bi = bx >> 3;
    const int rt = xcd * 4 + (bi >> 3), ct = bi & 7;
    const bool isF = wid >= 6;
    int k0, k1;
    if (isF) { k0 = (wid - 6) * 43; k1 = k0 + 43; } else { k0 = wid * 43 - (wid > 4 ? wid - 4 : 0); k1 = k0 + (wid < 4 ? 43 : 42); }
    const int nks = isF ? NKS_H : 256;
    const char* bsrc = (const char*)(isF ? HF : ctxF);
    const __amdgpu_buffer_rsrc_t srdB0 = mk_srd(bsrc + (size_t)(2 * rt) * nks * 1024, (unsigned)k1 * 1024u);
    const __amdgpu_buffer_rsrc_t srdB1 = mk_srd(bsrc + (size_t)(2 * rt + 1) * nks * 1024, (unsigned)k1 * 1024u);
    const __amdgpu_buffer_rsrc_t srdW = mk_srd((isF ? wd : wo) + ct * 64, (unsigned)k1 * 16u * 1024u - ct * 128);
    const unsigned vo16 = (unsigned)(lane * 16);
    const unsigned voW = (unsigned)(((lane >> 2) & 7) * 1024 + (lane >> 5) * 64 + (lane & 3) * 16);
    const unsigned m0w = lds0 + wid * 16384;
    const int trq = (lane & 15) >> 2, trp = lane & 3, trb = (lane >> 4) & 1;
    const lds_cptr rp = lds + wid * 16384 + (4 * hi + trq) * 64 + trb * 32 + trp * 8;
    f32x16 acc[2][2];
    acc[0][0] = f32x16{}; acc[0][1] = f32x16{}; acc[1][0] = f32x16{}; acc[1][1] = f32x16{};
    bf16x8 bf[8][2];
#define ISSUE(slot, ks) do { const unsigned k_ = (unsigned)(ks); \
        dma16(m0w + (slot) * 2048, voW, srdW, k_ * 16384u); dma16(m0w + (slot) * 2048 + 1024, voW, srdW, k_ * 16384u + 8192u); \
        asm volatile("buffer_load_dwordx4 %0, %1, %2, %3 offen" : "=v"(bf[slot][0]) : "v"(vo16), "s"(srdB0), "s"(k_ * 1024u) : "memory"); \
        asm volatile("buffer_load_dwordx4 %0, %1, %2, %3 offen" : "=v"(bf[slot][1]) : "v"(vo16), "s"(srdB1), "s"(k_ * 1024u) : "memory"); } while (0)
    asm volatile("s_nop 4");
#pragma unroll
    for (int i = 0; i < 7; ++i) ISSUE(i, k0 + i);
    for (int it = 0; it < 6; ++it) {
#pragma unroll
        for (int i = 0; i < 8; ++i) {
            const int ks = k0 + it * 8 + i;
            ISSUE((i + 7) & 7, ks + 7);
            asm volatile("s_waitcnt vmcnt(28)" : "+v"(bf[i][0]), "+v"(bf[i][1])::"memory");
            const lds_cptr p = rp + i * 2048;
            const bf16x8 w0 = cat8(lds_tr(p), lds_tr(p + 1024)), w1 = cat8(lds_tr(p + 512), lds_tr(p + 1536));
            acc[0][0] = MFMA(w0, bf[i][0], acc[0][0]); acc[0][1] = MFMA(w0, bf[i][1], acc[0][1]);
            acc[1][0] = MFMA(w1, bf[i][0], acc[1][0]); acc[1][1] = MFMA(w1, bf[i][1], acc[1][1]);
            SBAR();
        }
    }
#undef ISSUE
    asm volatile("s_waitcnt vmcnt(0)" : "+v"(bf[0][0]), "+v"(bf[0][1]), "+v"(bf[1][0]), "+v"(bf[1][1]), "+v"(bf[2][0]), "+v"(bf[2][1]), "+v"(bf[3][0]), "+v"(bf[3][1]),
                 "+v"(bf[4][0]), "+v"(bf[4][1]), "+v"(bf[5][0]), "+v"(bf[5][1]), "+v"(bf[6][0]), "+v"(bf[6][1]), "+v"(bf[7][0]), "+v"(bf[7][1])::"memory");
    STAMP(3, 1);
#pragma unroll
    for (int nb = 0; nb < 2; ++nb)
#pragma unroll
        for (int q = 0; q < 2; ++q)
#pragma unroll
            for (int g = 0; g < 4; ++g) {
                const f32x4 v = {acc[nb][q][4 * g], acc[nb][q][4 * g + 1], acc[nb][q][4 * g + 2], acc[nb][q][4 * g + 3]};
                *(LAS f32x4*)(lds + wid * 16384 + (((nb * 2 + q) * 4 + g) * 64 + lane) * 16) = v;
            }
    __syncthreads();
#pragma unroll
    for (int uu = 0; uu < 2; ++uu) {
        const int unit = wid * 2 + uu, blk = unit >> 2, g = unit & 3, nb = blk >> 1, q = blk & 1;
        f32x4 sv = {0.f, 0.f, 0.f, 0.f}, fv = {0.f, 0.f, 0.f, 0.f};
#pragma unroll
        for (int w = 0; w < 6; ++w) sv += *(const LAS f32x4*)(lds + w * 16384 + (unit * 64 + lane) * 16);
#pragma unroll
        for (int w = 6; w < 8; ++w) fv += *(const LAS f32x4*)(lds + w * 16384 + (unit * 64 + lane) * 16);
        const int row = rt * 64 + q * 32 + r32, col = ct * 64 + nb * 32 + g * 8 + 4 * hi;
        const u32x2 xr = *(const u32x2*)(x + (size_t)row * DM + col);
        float xv[4] = {__uint_as_float(xr[0] << 16), __uint_as_float(xr[0] & 0xffff0000u), __uint_as_float(xr[1] << 16), __uint_as_float(xr[1] & 0xffff0000u)};
        float ov[4];
#pragma unroll
        for (int e = 0; e < 4; ++e) {
            const float f = fv[e];
            const float sg = f * __builtin_amdgcn_rcpf(1.f + __builtin_amdgcn_exp2f(-1.4426950409f * f));
            ov[e] = xv[e] + sv[e] * sg;
        }
        u32x2 w; w[0] = cvtpk(ov[0], ov[1]); w[1] = cvtpk(ov[2], ov[3]);
        *(u32x2*)(out + (size_t)row * DM + col) = w;
    }
    STAMP(3, 2);
}

extern "C" void kernel_launch(void* const* d_in, const int* in_sizes, int n_in, void* d_out, int out_size, void* d_ws, size_t ws_size,
                              hipStream_t stream) {
    const u16* x = (const u16*)d_in[0];
    const u16* ckv = (const u16*)d_in[1];
    const u16* wq = (const u16*)d_in[2];
    const u16* wuk = (const u16*)d_in[3];
    const u16* wuv = (const u16*)d_in[4];
    const u16* wo = (const u16*)d_in[5];
    const u16* wg = (const u16*)d_in[7];
    const u16* wu = (const u16*)d_in[8];
    const u16* wd = (const u16*)d_in[9];
    char* ws = (char*)d_ws;
    u16* xnF = (u16*)(ws + WS_XN); u16* KF = (u16*)(ws + WS_K); u16* VF = (u16*)(ws + WS_V); u16* ctxF = (u16*)(ws + WS_CTX); u16* HF = (u16*)(ws + WS_H);
    (void)hipFuncSetAttribute((const void*)k_proj, hipFuncAttributeMaxDynamicSharedMemorySize, 131072);
    (void)hipFuncSetAttribute((const void*)k_attn, hipFuncAttributeMaxDynamicSharedMemorySize, LDS_ATT);
    (void)hipFuncSetAttribute((const void*)k_out, hipFuncAttributeMaxDynamicSharedMemorySize, 131072);
    k_ln<<<256, 512, 0, stream>>>(x, xnF);
    k_proj<<<256, 512, 131072, stream>>>(ckv, wuk, wuv, xnF, wg, wu, KF, VF, HF TSPASS);
    k_attn<<<256, 512, LDS_ATT, stream>>>(xnF, wq, KF, VF, ctxF TSPASS);
    k_out<<<256, 512, 131072, stream>>>(ctxF, HF, wo, wd, x, (u16*)d_out TSPASS);
}
```

```cpp
#include <hip/hip_runtime.h>
#include <stdint.h>
#include <utility>

typedef short bf16x8 __attribute__((ext_vector_type(8)));
typedef short s16x4 __attribute__((ext_vector_type(4)));
typedef float f32x16 __attribute__((ext_vector_type(16)));
typedef float f32x4 __attribute__((ext_vector_type(4)));
typedef float f32x2 __attribute__((ext_vector_type(2)));
typedef __bf16 bf16x2_t __attribute__((ext_vector_type(2)));
typedef unsigned u32x4 __attribute__((ext_vector_type(4)));
typedef unsigned u32x2 __attribute__((ext_vector_type(2)));
typedef unsigned short u16;
typedef unsigned long long u64;
#define LAS __attribute__((address_space(3)))
#define GAS __attribute__((address_space(1)))
typedef LAS const char* lds_cptr;
typedef LAS char* lds_ptr;
typedef __amdgpu_buffer_rsrc_t srd_t;
#define DI __device__ __forceinline__
#define SBAR() __builtin_amdgcn_sched_barrier(0)
#define PIN(x) asm volatile("" : "+v"(x))
#define MFMA(a, b, c) __builtin_amdgcn_mfma_f32_32x32x16_bf16((a), (b), (c), 0, 0, 0)
template <int N> using IC = std::integral_constant<int, N>;

#define STAMPL(i) do {} while (0)
#define STAMPR(i) do {} while (0)
constexpr int DM = 512, HD = 128, FF = 1376;
constexpr size_t MiB = 1u << 20;
constexpr unsigned KSTR = 65 * 1024;
constexpr size_t WS_SEED = 0, WS_FX = 4096, WS_FKV = 8192, WS_FH = 24576, WS_FC = 40960;
constexpr size_t WS_XN = 1 * MiB, WS_KV = 4 * MiB, WS_CTX = 36 * MiB, WS_H = WS_CTX + (size_t)256 * KSTR, WS_TS = 64 * MiB;
constexpr int LDS_BYTES = 163840, L_WA = 0, L_WB = 106496;
constexpr float QSCALE = 0.08838834764831845f * 1.4426950408889634f;
constexpr float THR = 8.f;

DI unsigned rfl(unsigned x) { return __builtin_amdgcn_readfirstlane(x); }
DI unsigned cvtpk(float lo, float hi) { f32x2 v = {lo, hi}; bf16x2_t b = __builtin_convertvector(v, bf16x2_t); return __builtin_bit_cast(unsigned, b); }
DI bf16x8 lds_rd128(lds_cptr p) { return *(const LAS bf16x8*)p; }
typedef short v4i16_t __attribute__((ext_vector_type(4)));
DI s16x4 lds_tr(lds_cptr p) { return __builtin_bit_cast(s16x4, __builtin_amdgcn_ds_read_tr16_b64_v4i16((LAS v4i16_t*)p)); }
DI bf16x8 cat8(s16x4 a, s16x4 b) { return __builtin_shufflevector(a, b, 0, 1, 2, 3, 4, 5, 6, 7); }
DI lds_cptr lp(unsigned a) { return (lds_cptr)(uintptr_t)a; }
DI srd_t mk_srd(const void* p, unsigned bytes) { return __builtin_amdgcn_make_buffer_rsrc((void*)p, (short)0, (int)bytes, 0x00020000); }
DI void dma16(unsigned m0v, unsigned voff, srd_t srd, unsigned soff) {
    asm volatile("s_mov_b32 m0, %0\n\ts_nop 0\n\tbuffer_load_dwordx4 %1, %2, %3 offen lds" ::"s"(m0v), "v"(voff), "s"(srd), "s"(soff) : "memory");
}
DI void dma16x4(unsigned m0v, unsigned voff, srd_t srd, unsigned soff) {
    asm volatile("s_mov_b32 m0, %0\n\ts_nop 0\n\tbuffer_load_dwordx4 %1, %2, %3 offen sc1 lds\n\tbuffer_load_dwordx4 %1, %2, %3 offen offset:1024 sc1 lds\n\t"
                 "buffer_load_dwordx4 %1, %2, %3 offen offset:2048 sc1 lds\n\tbuffer_load_dwordx4 %1, %2, %3 offen offset:3072 sc1 lds"
                 ::"s"(m0v), "v"(voff), "s"(srd), "s"(soff) : "memory");
}
DI void dma16x2(unsigned m0v, unsigned voff, srd_t srd, unsigned soff) {
    asm volatile("s_mov_b32 m0, %0\n\ts_nop 0\n\tbuffer_load_dwordx4 %1, %2, %3 offen sc1 lds\n\tbuffer_load_dwordx4 %1, %2, %3 offen offset:1024 sc1 lds"
                 ::"s"(m0v), "v"(voff), "s"(srd), "s"(soff) : "memory");
}
template <int OFF> DI void bufld16(bf16x8& dst, unsigned voff, srd_t srd, unsigned soff) {
    asm volatile("buffer_load_dwordx4 %0, %1, %2, %3 offen offset:%4 sc1" : "=v"(dst) : "v"(voff), "s"(srd), "s"(soff), "i"(OFF) : "memory");
}
DI void st16_wt(u32x4 v, srd_t srd, unsigned voff, unsigned soff) {
    asm volatile("buffer_store_dwordx4 %0, %1, %2, %3 offen sc1\n\ts_nop 1" ::"v"(v), "v"(voff), "s"(srd), "s"(soff) : "memory");
}
template <int N> DI void warm(u32x4& dst, const void* base, unsigned bytes, unsigned gw, unsigned vo16) {
    const srd_t srd = mk_srd(base, bytes);
#pragma unroll
    for (int i = 0; i < N; ++i)
        asm volatile("buffer_load_dwordx4 %0, %1, %2, %3 offen sc1" : "+v"(dst) : "v"(vo16), "s"(srd), "s"((gw * N + i) * 1024u) : "memory");
}
#define WAITV(n) asm volatile("s_waitcnt vmcnt(" #n ")" ::: "memory")
template <int N, class T> DI void waitv(T& x) { asm volatile("s_waitcnt vmcnt(%1)" : "+v"(x) : "n"(N) : "memory"); }
#define BARRIER() asm volatile("s_barrier" ::: "memory")
DI u32x4 pack8(const f32x16& x, int s, float sc) {
    u32x4 w;
    w[0] = cvtpk(x[8 * s + 0] * sc, x[8 * s + 1] * sc); w[1] = cvtpk(x[8 * s + 2] * sc, x[8 * s + 3] * sc);
    w[2] = cvtpk(x[8 * s + 4] * sc, x[8 * s + 5] * sc); w[3] = cvtpk(x[8 * s + 6] * sc, x[8 * s + 7] * sc);
    return w;
}
DI u32x4 pack8n(const f32x16& x, int s) {
    u32x4 w;
    w[0] = cvtpk(x[8 * s + 0], x[8 * s + 1]); w[1] = cvtpk(x[8 * s + 2], x[8 * s + 3]);
    w[2] = cvtpk(x[8 * s + 4], x[8 * s + 5]); w[3] = cvtpk(x[8 * s + 6], x[8 * s + 7]);
    return w;
}
template <int... Is, class F> DI void static_for_(std::integer_sequence<int, Is...>, F&& f) { (f(IC<Is>{}), ...); }
template <int N, class F> DI void static_for(F&& f) { static_for_(std::make_integer_sequence<int, N>{}, f); }

DI void flag_set(srd_t ctl, unsigned soff, u64 tag) {
    const u32x2 v = {(unsigned)tag, (unsigned)(tag >> 32)};
    asm volatile("buffer_store_dwordx2 %0, off, %1, %2 sc1\n\ts_nop 0" ::"v"(v), "s"(ctl), "s"(soff) : "memory");
}
DI void poll_issue(u32x2& v, srd_t ctl, unsigned voff, unsigned soff) { asm volatile("buffer_load_dwordx2 %0, %1, %2, %3 offen sc1" : "=v"(v) : "v"(voff), "s"(ctl), "s"(soff) : "memory"); }
DI void poll_finish(u32x2 v, srd_t ctl, unsigned voff, unsigned soff, bool need, u64 tag) {
    if (__builtin_expect(__builtin_amdgcn_ballot_w64(need && (((u64)v[1] << 32) | v[0]) != tag) != 0, 0)) {
        const u64 t0 = __builtin_amdgcn_s_memrealtime();
        for (;;) {
            __builtin_amdgcn_s_sleep(1);
            asm volatile("buffer_load_dwordx2 %0, %1, %2, %3 offen sc1\n\ts_waitcnt vmcnt(0)" : "=&v"(v) : "v"(voff), "s"(ctl), "s"(soff) : "memory");
            if (__builtin_amdgcn_ballot_w64(need && (((u64)v[1] << 32) | v[0]) != tag) == 0) break;
            if (__builtin_amdgcn_s_memrealtime() - t0 > 400000ull) break;
        }
    }
    asm volatile("" ::: "memory");
}
DI int lane_id() { int l; asm volatile("v_mbcnt_lo_u32_b32 %0, -1, 0\n\tv_mbcnt_hi_u32_b32 %0, -1, %0" : "=v"(l)); return l; }

#define MX3(a, b, c) __builtin_fmaxf(__builtin_fmaxf((a), (b)), (c))
#define EX(v) __builtin_amdgcn_exp2f(v)
struct AttSt {
    f32x16 o[4]; bf16x8 qf[8]; bf16x8 fr[8]; f32x16 negm;
    float l, m;
};
DI void cmask(f32x16& p, int dq) {
#pragma unroll
    for (int r = 0; r < 16; ++r) { const int c = (r & 3) + 8 * (r >> 2); p[r] = (c > dq) ? -__builtin_inff() : p[r]; }
}
template <bool DMA, bool MASK, bool DOQK>
DI void half_step(AttSt& s, f32x16& Sn, f32x16& Sc, u32x4 (&Pc)[2], u32x4 (&Pp)[2], lds_cptr kp, lds_cptr vp, lds_cptr np, lds_cptr vc, int dq,
                  unsigned dm0, unsigned dvo, srd_t dsrd, unsigned dso) {
    float r0, r1, ma, mb;
#define RD1(n) s.fr[(n) & 7] = lds_rd128(((n) < 8 ? kp : (n) < 16 ? vp : np) + ((n) & 7) * 1024)
#define RD(i) do { if constexpr (DOQK || (i) + 6 >= 8) { RD1((i) + 7); RD1((i) + 6); } } while (0)
#define QK(i) do { if constexpr (DOQK) Sn = MFMA(s.fr[i], s.qf[i], (i) == 0 ? s.negm : Sn); } while (0)
#define PV(i) s.o[(i) & 3] = MFMA(s.fr[i], __builtin_bit_cast(bf16x8, Pp[(i) >> 2]), s.o[(i) & 3])
#define E(i) Sc[i] = EX(Sc[i])
#define C(i) Pc[(i) >> 2][(i) & 3] = cvtpk(Sc[2 * (i)], Sc[2 * (i) + 1])
#define ENDG() do { PIN(Sc); SBAR(); } while (0)
    SBAR();
    QK(0); RD(0); E(0); ENDG();
    QK(1); E(1); E(2); E(3); ENDG();
    QK(2); RD(2); E(4); C(0); PIN(Pc[0]); ENDG();
    QK(3); E(5); E(6); r0 = Sc[0] + Sc[2]; r1 = Sc[1] + Sc[3]; PIN(r0); PIN(r1); ENDG();
    QK(4); RD(4); E(7); C(1); PIN(Pc[0]); ENDG();
    QK(5); E(8); r0 += Sc[4]; r1 += Sc[5]; C(2); r0 += Sc[6]; PIN(r0); PIN(r1); PIN(Pc[0]); ENDG();
    QK(6); RD(6); E(9); r1 += Sc[7]; PIN(r1); ENDG();
    QK(7); E(10); E(11); C(3); r0 += Sc[8]; PIN(r0); PIN(Pc[0]); ENDG();
    PV(0); RD(8); E(12); r1 += Sc[9]; PIN(r1); ENDG();
    if constexpr (DMA) { dma16x4(dm0, dvo, dsrd, dso); SBAR(); }
    PV(1); E(13); C(4); r0 += Sc[10]; r1 += Sc[11]; C(5); PIN(r0); PIN(r1); PIN(Pc[1]); ENDG();
    if constexpr (MASK) { cmask(Sn, dq); SBAR(); }
    PV(2); RD(10); E(14); if constexpr (DOQK) { ma = MX3(Sn[0], Sn[1], Sn[2]); PIN(ma); } ENDG();
    PV(3); E(15); r0 += Sc[12]; r1 += Sc[13]; C(6); if constexpr (DOQK) { mb = MX3(Sn[3], Sn[4], Sn[5]); PIN(mb); } PIN(r0); PIN(r1); PIN(Pc[1]); ENDG();
    PV(4); RD(12); r0 += Sc[14]; if constexpr (DOQK) { ma = MX3(ma, Sn[6], Sn[7]); mb = MX3(mb, Sn[8], Sn[9]); PIN(ma); PIN(mb); } PIN(r0); ENDG();
    PV(5); r1 += Sc[15]; C(7); if constexpr (DOQK) { ma = MX3(ma, Sn[10], Sn[11]); mb = MX3(mb, Sn[12], Sn[13]); ma = MX3(ma, Sn[14], Sn[15]); PIN(ma); PIN(mb); } PIN(r1); PIN(Pc[1]); SBAR();
    PV(6); RD(14); r0 += r1; s.l += r0; PIN(s.l); SBAR();
    PV(7);
    if constexpr (DOQK) {
        ma = MX3(ma, mb, Sn[15]);
        if (__builtin_expect(__builtin_amdgcn_ballot_w64(ma > THR) != 0, 0)) {
            auto rr = __builtin_amdgcn_permlane32_swap(__float_as_uint(ma), __float_as_uint(ma), false, false);
            const float dl = __builtin_fmaxf(__builtin_fmaxf(__uint_as_float(rr[0]), __uint_as_float(rr[1])), 0.f);
#pragma unroll
            for (int i = 0; i < 8; ++i) { const bf16x8 f = lds_rd128(vc + i * 1024); s.o[i & 3] = MFMA(f, __builtin_bit_cast(bf16x8, Pc[i >> 2]), s.o[i & 3]); }
            const float f = EX(-dl);
            s.m += dl; s.l *= f;
#pragma unroll
            for (int d = 0; d < 4; ++d)
#pragma unroll
                for (int r = 0; r < 16; ++r) s.o[d][r] *= f;
#pragma unroll
            for (int r = 0; r < 16; ++r) { Sn[r] -= dl; s.negm[r] = -s.m; }
            Pc[0] = u32x4{0, 0, 0, 0}; Pc[1] = u32x4{0, 0, 0, 0};
            PIN(s.negm); PIN(Sn); PIN(Pc[0]); PIN(Pc[1]);
        }
    }
    SBAR();
#undef RD1
#undef RD
#undef QK
#undef PV
#undef E
#undef C
#undef ENDG
}

struct Args { const u16 *x, *ckv, *wq, *wuk, *wuv, *wo, *wg, *wu, *wd; u16* out; char* ws; };

template <bool HI>
DI void run(const Args& A, lds_ptr lds, const int wid) {
    STAMPL(0); STAMPR(14);
    const unsigned lds0 = (unsigned)(uintptr_t)lds;
    const int lane = threadIdx.x & 63, r32 = lane & 31, hi = lane >> 5, w4 = wid & 3;
    const int bx = blockIdx.x, xcd = bx & 7, bi = bx >> 3;
    const int h = xcd + 8 * (bi >> 3), j = bi & 7;
    const int trq = (lane & 15) >> 2, trp = lane & 3, trb = (lane >> 4) & 1;
    const unsigned vo16 = (unsigned)(lane * 16);
    char* const ws = A.ws;
    const u64 seed = *(const u64*)(ws + WS_SEED);
    const u64 tag = seed + 1;
    const srd_t ctl = mk_srd(ws, 65536);
    const int qb = HI ? j : 15 - j, qg = qb * 4 + w4;
    const int NT = 2 * (15 - j) + 2;
    const srd_t srdKV = mk_srd(ws + WS_KV + (size_t)h * 32 * 32768, (unsigned)NT * 32768u);
    const int u0 = h < 22 ? 3 * h : 66 + 2 * (h - 22), nu = h < 22 ? 3 : 2;
    const srd_t srdW = mk_srd(A.wq + h * HD, DM * 4096 * 2 - h * HD * 2);
    const srd_t srdF = mk_srd((HI ? A.wu : A.wg) + u0 * 16, DM * FF * 2 - u0 * 32);
    const srd_t srdX = mk_srd(ws + WS_XN + qg * 1024, 32 * KSTR - qg * 1024);
    const unsigned voW = (unsigned)((((lane >> 2) & 7) + wid * 16) * 8192 + (lane >> 5) * 64 + (lane & 3) * 16);
    const unsigned m0W = lds0 + wid * 4096, m0F = lds0 + 32768 + wid * 3072;
    bf16x8 xf[8];
    s16x4 wl[5], wh[5];
    u32x4 kvp[16];
    unsigned voF[3];
    auto dmaW = [&](int c, int slot, auto ic) {
        constexpr int i = decltype(ic)::value;
        if constexpr (i < 4) dma16(m0W + slot + i * 1024, voW, srdW, (unsigned)((c * 128 + (i >> 1) * 8) * 8192 + (i & 1) * 128));
        else if constexpr (i < 7) dma16(m0F + slot + (i - 4) * 1024, voF[i - 4], srdF, (unsigned)(c * 128 * FF * 2));
    };
    auto ldX = [&](int c, auto kc) { constexpr int k = decltype(kc)::value; bufld16<0>(xf[k], vo16, srdX, (unsigned)(c * 8 + k) * KSTR); };
    unsigned bW, bF0, bF1;
    auto rdW = [&](auto mc) {
        constexpr int m = decltype(mc)::value, ks = (m / 7) & 7, b = m % 7, e = m % 5;
        if constexpr (b < 4) { wl[e] = lds_tr(lp(bW) + ks * 4096 + b * 512); wh[e] = lds_tr(lp(bW) + ks * 4096 + b * 512 + 256); }
        else { wl[e] = lds_tr(lp(bF0) + ks * 1536 + (b - 4) * 32); wh[e] = lds_tr(lp(bF1) + ks * 1536 + (b - 4) * 32); }
    };

    {
        const int wr = wid & 1, wc1 = (wid >> 1) & 1;
        const srd_t srdA = mk_srd(A.ckv + (size_t)j * 256 * DM, 256 * DM * 2);
        const srd_t srdB = mk_srd((HI ? A.wuv : A.wuk) + h * HD, DM * 4096 * 2 - h * HD * 2);
        unsigned voA[2];
#pragma unroll
        for (int par = 0; par < 2; ++par) voA[par] = (unsigned)((32 * wid + (lane >> 3)) * 1024 + (((lane & 7) ^ (4 * par + (lane >> 4))) * 16));
        const unsigned voB = (unsigned)((((lane >> 2) & 7) + w4 * 16) * 8192 + (lane >> 5) * 64 + (lane & 3) * 16);
        const unsigned m0A = lds0 + wid * 4096, m0B = lds0 + 32768 + wid * 4096;
        auto dma = [&](int kt0, int slot, auto ic) {
            constexpr int i = decltype(ic)::value;
            const int kt = (kt0 + xcd) & 7;
            if constexpr (i < 4) dma16(m0A + slot * 65536 + i * 1024, voA[i & 1], srdA, (unsigned)(kt * 128 + i * 8192));
            else dma16(m0B + slot * 65536 + (i - 4) * 1024, voB, srdB, (unsigned)(kt * 64 * 8192 + ((i - 4) >> 1) * 8 * 8192 + (i & 1) * 128));
        };
        const int row = bx * 8 + wid;
        u32x4 raw;
        asm volatile("s_nop 4\n\tglobal_load_dwordx4 %0, %1, %2" : "=v"(raw) : "v"(vo16), "s"(A.x + (size_t)row * DM) : "memory");
        static_for<8>([&](auto ic) { dma(0, 0, ic); });
        static_for<8>([&](auto ic) { dma(1, 1, ic); });
        {
            waitv<16>(raw);
            float v[8];
#pragma unroll
            for (int i = 0; i < 4; ++i) { v[2 * i] = __uint_as_float(raw[i] << 16); v[2 * i + 1] = __uint_as_float(raw[i] & 0xffff0000u); }
            float s = 0.f;
#pragma unroll
            for (int i = 0; i < 8; ++i) s += v[i];
#pragma unroll
            for (int o = 1; o < 64; o <<= 1) s += __shfl_xor(s, o);
            const float mean = s * (1.f / DM);
            float q = 0.f;
#pragma unroll
            for (int i = 0; i < 8; ++i) { v[i] -= mean; q += v[i] * v[i]; }
#pragma unroll
            for (int o = 1; o < 64; o <<= 1) q += __shfl_xor(q, o);
            const float rstd = 1.f / sqrtf(q * (1.f / DM) + 1e-5f);
            u32x4 w;
#pragma unroll
            for (int i = 0; i < 4; ++i) w[i] = cvtpk(v[2 * i] * rstd, v[2 * i + 1] * rstd);
            st16_wt(w, mk_srd(ws + WS_XN, 32 * KSTR), (unsigned)((lane >> 1) * KSTR + (lane & 1) * 512), (unsigned)((row >> 5) * 1024 + (row & 31) * 16));
        }
        unsigned aoff[4];
#pragma unroll
        for (int ks = 0; ks < 4; ++ks) { aoff[ks] = lds0 + (unsigned)((wr * 128 + r32) * 128 + (((2 * ks + hi) ^ ((r32 >> 1) & 7)) * 16)); PIN(aoff[ks]); }
        unsigned boff = lds0 + (unsigned)(32768 + (HI ? 16384 : 0) + wc1 * 1024 + hi * 2048 + trq * 64 + trb * 32 + trp * 8);
        PIN(boff);
        f32x16 acc[4][2];
#pragma unroll
        for (int a = 0; a < 4; ++a)
#pragma unroll
            for (int b = 0; b < 2; ++b) acc[a][b] = f32x16{};
        bf16x8 cf[2][4]; s16x4 kl[2][2], kh[2][2];
        auto rd = [&](int buf, int ks, auto ic) {
            constexpr int i = decltype(ic)::value;
            if constexpr (i == 0) kl[buf][0] = lds_tr(lp(boff) + ks * 4096);
            if constexpr (i == 1) kh[buf][0] = lds_tr(lp(boff) + ks * 4096 + 256);
            if constexpr (i == 2) cf[buf][0] = lds_rd128(lp(aoff[ks]));
            if constexpr (i == 3) kl[buf][1] = lds_tr(lp(boff) + ks * 4096 + 512);
            if constexpr (i == 4) kh[buf][1] = lds_tr(lp(boff) + ks * 4096 + 768);
            if constexpr (i >= 5) cf[buf][i - 4] = lds_rd128(lp(aoff[ks]) + (i - 4) * 4096);
        };
        auto mm = [&](int buf, auto ic) {
            constexpr int i = decltype(ic)::value;
            const bf16x8 w_ = cat8(kl[buf][i & 1], kh[buf][i & 1]);
            acc[i >> 1][i & 1] = HI ? MFMA(cf[buf][i >> 1], w_, acc[i >> 1][i & 1]) : MFMA(w_, cf[buf][i >> 1], acc[i >> 1][i & 1]);
        };
        WAITV(9); BARRIER();
        STAMPL(1);
        static_for<8>([&](auto ic) { rd(0, 0, ic); });
        SBAR();
#define KV_HEAD(WN) do { \
        static_for<8>([&](auto ic) { mm(0, ic); rd(1, 1, ic); SBAR(); }); \
        static_for<8>([&](auto ic) { mm(1, ic); rd(0, 2, ic); SBAR(); }); \
        static_for<8>([&](auto ic) { mm(0, ic); rd(1, 3, ic); SBAR(); }); \
        boff ^= 65536u; aoff[0] ^= 65536u; aoff[1] ^= 65536u; aoff[2] ^= 65536u; aoff[3] ^= 65536u; \
        asm volatile("s_waitcnt vmcnt(" #WN ") lgkmcnt(0)\n\ts_barrier" : "+v"(boff), "+v"(aoff[0]), "+v"(aoff[1]), "+v"(aoff[2]), "+v"(aoff[3])::"memory"); SBAR(); } while (0)
#define KV_TAIL(kt) static_for<8>([&](auto ic) { mm(1, ic); rd(0, 0, ic); dma((kt) + 2, (kt) & 1, ic); SBAR(); })
        u32x4 wsink = {0, 0, 0, 0};
        KV_HEAD(0);
        if (wid == 0) flag_set(ctl, (unsigned)(WS_FX + bx * 8), tag);
        KV_TAIL(0);
        {
            const unsigned gw = bx * 8 + wid;
            warm<2>(wsink, A.wq, DM * 4096 * 2, gw, vo16); warm<1>(wsink, A.wg, DM * FF * 2, gw, vo16); warm<1>(wsink, A.wu, DM * FF * 2, gw, vo16);
        }
        KV_HEAD(4); KV_TAIL(1);
        KV_HEAD(0); PIN(wsink); KV_TAIL(2);
#pragma unroll 1
        for (int kt = 3; kt < 5; ++kt) { KV_HEAD(0); KV_TAIL(kt); }
        u32x2 pv;
        const unsigned pxf = (unsigned)((((lane & 16) ? 15 - j : j) * 16 + (lane & 15)) * 8);
        if constexpr (!HI) { if (wid == 0) poll_issue(pv, ctl, pxf, (unsigned)WS_FX); }
        KV_HEAD(0);
        if constexpr (!HI) { if (wid == 0) { PIN(pv); poll_finish(pv, ctl, pxf, (unsigned)WS_FX, true, tag); } }
        KV_TAIL(5);
#pragma unroll
        for (int i = 0; i < 3; ++i) { const int g = (3 * w4 + i) * 64 + lane, r = g / 6, c = g - 6 * r; voF[i] = (unsigned)((HI ? (r ^ 4) : r) * (FF * 2) + c * 16); }
        KV_HEAD(0);
        static_for<8>([&](auto ic) { mm(1, ic); rd(0, 0, ic); dmaW(0, L_WA, ic); ldX(0, ic); SBAR(); });
        bW = lds0 + L_WA + (unsigned)(hi * 2048 + trq * 64 + trb * 32 + trp * 8);
        bF0 = lds0 + L_WA + (unsigned)(32768 + trb * 12288 + hi * 768 + (trq + 4 * trb) * 96 + trp * 8);
        bF1 = lds0 + L_WA + (unsigned)(32768 + trb * 12288 + hi * 768 + (trq + 4 - 4 * trb) * 96 + trp * 8);
        PIN(bW); PIN(bF0); PIN(bF1);
        static_for<8>([&](auto ic) { mm(0, ic); rd(1, 1, ic); SBAR(); });
        static_for<8>([&](auto ic) { mm(1, ic); rd(0, 2, ic); SBAR(); });
        static_for<8>([&](auto ic) { mm(0, ic); rd(1, 3, ic); SBAR(); });
        asm volatile("s_waitcnt vmcnt(0) lgkmcnt(0)\n\ts_barrier" : "+v"(xf[0]), "+v"(xf[1]), "+v"(xf[2]), "+v"(xf[3]), "+v"(xf[4]), "+v"(xf[5]), "+v"(xf[6]), "+v"(xf[7])::"memory");
        SBAR();
        static_for<8>([&](auto ic) { constexpr int i = decltype(ic)::value; mm(1, ic); if constexpr (i < 4) rdW(ic); dmaW(1, L_WB, ic); SBAR(); });
#undef KV_HEAD
#undef KV_TAIL
#pragma unroll
        for (int i = 0; i < 16; ++i) { kvp[i] = pack8n(acc[i >> 2][(i >> 1) & 1], i & 1); PIN(kvp[i]); }
        STAMPL(2);
    }
    AttSt s;
    {
        const int wr = wid & 1, wc1 = (wid >> 1) & 1;
        const srd_t srdO = mk_srd(ws + WS_KV + (size_t)h * 32 * 32768 + (HI ? 16384 : 0), 32 * 32768);
        auto stKV = [&](auto ic) {
            constexpr int i = decltype(ic)::value, kb = i >> 2, db = (i >> 1) & 1, sh = i & 1;
            const int u = j * 8 + wr * 4 + kb, dblk = 2 * wc1 + db, fi = HI ? (sh * 4 + dblk) : (2 * dblk + sh);
            st16_wt(kvp[i], srdO, vo16, (unsigned)((u >> 1) * 32768 + (u & 1) * 8192 + fi * 1024));
        };
        const int smode = j >> 1;
        const unsigned pkv = (unsigned)(WS_FKV + (h * 64 + j * 8 + wid) * 8);
        f32x16 acc[7];
#pragma unroll
        for (int d = 0; d < 7; ++d) acc[d] = f32x16{};
        u32x2 pv;
        static_for<224>([&](auto mc) {
            constexpr int m = decltype(mc)::value, c = m / 56, k = (m / 7) & 7, b = m % 7, r = m % 56;
            if constexpr (b == 0 && c > 0) { constexpr int wn = c < 3 ? (k < 7 ? 14 : 7) : (k == 0 ? 7 : 17 - k); waitv<wn>(xf[k]); SBAR(); }
            if constexpr (r == 52 && c < 3) {
                constexpr unsigned dl = (c & 1) ? (unsigned)(L_WA - L_WB) : (unsigned)(L_WB - L_WA);
                bW += dl; bF0 += dl; bF1 += dl;
                asm volatile("s_waitcnt vmcnt(%3) lgkmcnt(0)\n\ts_barrier" : "+v"(bW), "+v"(bF0), "+v"(bF1) : "n"(c == 0 ? 7 : 8) : "memory");
                SBAR();
                STAMPL(3 + c);
            }
            acc[b] = MFMA(cat8(wl[m % 5], wh[m % 5]), xf[k], acc[b]);
            if constexpr (m + 4 < 224) rdW(IC<m + 4>{});
            if constexpr (r >= 52 && c < 2) { dmaW(c + 2, (c & 1) ? L_WB : L_WA, IC<2 * (r - 52)>{}); dmaW(c + 2, (c & 1) ? L_WB : L_WA, IC<2 * (r - 52) + 1>{}); }
            if constexpr (c == 3 && r < 2) dma16x4(lds0 + wid * 10240 + r * 4096, vo16, srdKV, (unsigned)(wid * 10240 + r * 4096));
            if constexpr (c == 3 && r == 2) dma16x2(lds0 + wid * 10240 + 8192, vo16, srdKV, (unsigned)(wid * 10240 + 8192));
            if constexpr (b == 6) {
                PIN(acc[0]); PIN(acc[1]); PIN(acc[2]); PIN(acc[3]); PIN(acc[4]); PIN(acc[5]); PIN(acc[6]);
                if (smode == c) { stKV(IC<2 * k>{}); stKV(IC<2 * k + 1>{}); }
                if constexpr (c < 3) ldX(c + 1, IC<k>{});
                if constexpr (c > 0 && k == 4) { if (smode == c - 1) { if constexpr (c < 3) WAITV(6); else WAITV(11); flag_set(ctl, pkv, tag); } }
                if constexpr (!HI && c == 2 && k == 1) { if (wid == 0) poll_issue(pv, ctl, vo16 >> 1, (unsigned)(WS_FKV + h * 512)); }
                if constexpr (!HI && c == 2 && k == 4) { if (wid == 0) { waitv<3>(pv); poll_finish(pv, ctl, vo16 >> 1, (unsigned)(WS_FKV + h * 512), vo16 < 256, tag); } }
            }
            SBAR();
        });
        STAMPL(6);
#pragma unroll
        for (int d = 0; d < 4; ++d)
#pragma unroll
            for (int q = 0; q < 2; ++q) s.qf[2 * d + q] = __builtin_bit_cast(bf16x8, pack8(acc[d], q, QSCALE));
        u32x4 hw[3];
#pragma unroll
        for (int v = 0; v < 3; ++v) {
            float hv[8];
#pragma unroll
            for (int r = 0; r < 8; ++r) {
                const float g = acc[4 + v][r];
                const float z = g * (1.5957691216f + 0.0713548163f * g * g);
                const float e = __builtin_amdgcn_exp2f(-1.4426950409f * z);
                hv[r] = g * __builtin_amdgcn_rcpf(1.f + e) * acc[4 + v][8 + r];
            }
            hw[v] = u32x4{cvtpk(hv[0], hv[1]), cvtpk(hv[2], hv[3]), cvtpk(hv[4], hv[5]), cvtpk(hv[6], hv[7])};
        }
        asm volatile("s_waitcnt vmcnt(0) lgkmcnt(0)\n\ts_barrier" : "+v"(hw[0]), "+v"(hw[1]), "+v"(hw[2])::"memory");
        if (smode == 3) flag_set(ctl, pkv, tag);
        const srd_t srdH = mk_srd(ws + WS_H + (size_t)u0 * KSTR + qg * 1024, (unsigned)nu * KSTR - qg * 1024);
#pragma unroll
        for (int v = 0; v < 3; ++v) st16_wt(hw[v], srdH, vo16, (unsigned)v * KSTR);
    }
    STAMPL(7);
    bool cpub = false;
    const unsigned pcf = (unsigned)(WS_FC + (qg * 32 + h) * 8);
    {
        const int lane = lane_id(), r32 = lane & 31, hi = lane >> 5;
        const int tl = qg >> 1;
        const int row = qg * 32 + r32;
        const unsigned dvo = (unsigned)(lane * 16 + w4 * 4096);
        const unsigned m0b = lds0 + w4 * 4096;
        f32x16 Sa, Sb; u32x4 Pa[2], Pb[2];
        Pb[0] = u32x4{0, 0, 0, 0}; Pb[1] = u32x4{0, 0, 0, 0}; Pa[0] = Pb[0]; Pa[1] = Pb[0];
        unsigned P = lds0 + vo16, Q = lds0 + 65536 + vo16;
        PIN(P); PIN(Q);
        {
#pragma unroll
            for (int i = 0; i < 8; ++i) s.fr[i] = lds_rd128(lp(P) + i * 1024);
            SBAR();
            f32x16 z = f32x16{};
            static_for<8>([&](auto ic) { constexpr int i = decltype(ic)::value; z = MFMA(s.fr[i], s.qf[i], z);
                if constexpr (i == 1 || i == 3 || i == 5) { s.fr[i] = lds_rd128(lp(P) + 8192 + i * 1024); s.fr[i - 1] = lds_rd128(lp(P) + 8192 + (i - 1) * 1024); } SBAR(); });
            Sa = z;
            if (tl == 0) cmask(Sa, row - 4 * hi);
            float ma = MX3(Sa[0], Sa[1], Sa[2]), mb = MX3(Sa[3], Sa[4], Sa[5]);
            ma = MX3(ma, Sa[6], Sa[7]); mb = MX3(mb, Sa[8], Sa[9]); ma = MX3(ma, Sa[10], Sa[11]); mb = MX3(mb, Sa[12], Sa[13]); ma = MX3(ma, Sa[14], Sa[15]);
            ma = __builtin_fmaxf(ma, mb);
            auto rr = __builtin_amdgcn_permlane32_swap(__float_as_uint(ma), __float_as_uint(ma), false, false);
            const float rm = __builtin_fmaxf(__uint_as_float(rr[0]), __uint_as_float(rr[1]));
            s.m = rm; s.l = 0.f;
#pragma unroll
            for (int r = 0; r < 16; ++r) { Sa[r] -= rm; s.negm[r] = -rm; }
#pragma unroll
            for (int d = 0; d < 4; ++d) s.o[d] = f32x16{};
            PIN(s.negm);
        }
        bool hpub = false;
        const unsigned phf = (unsigned)(WS_FH + (qg * 32 + h) * 8);
#define STEP_END() do { if constexpr (HI) WAITV(8); if (!hpub) { if constexpr (!HI) WAITV(0); flag_set(ctl, phf, tag); hpub = true; } BARRIER(); } while (0)
        int t = 0;
        const int npair = (tl - 1) >> 1;
        if (npair > 0) {
            unsigned sP = m0b, sQ = m0b + 65536, sk = 3 * 32768u;
#pragma unroll 1
            for (int it = 0; it < npair; ++it) {
                half_step<HI, false, true>(s, Sb, Sa, Pa, Pb, lp(P) + 8192, lp(Q) + 57344, lp(P) + 32768, lp(P) + 16384, 0, sQ + 32768, dvo, srdKV, sk);
                half_step<HI, false, true>(s, Sa, Sb, Pb, Pa, lp(P) + 32768, lp(P) + 16384, lp(P) + 40960, lp(P) + 24576, 0, sQ + 16384, dvo, srdKV, sk - 16384u);
                u32x2 pv;
                if constexpr (!HI) { if (it == 1 && wid == 0) poll_issue(pv, ctl, vo16 >> 1, (unsigned)(WS_FKV + h * 512)); }
                STEP_END();
                half_step<HI, false, true>(s, Sb, Sa, Pa, Pb, lp(P) + 40960, lp(P) + 24576, lp(Q), lp(P) + 49152, 0, sP, dvo, srdKV, sk + 32768u);
                half_step<HI, false, true>(s, Sa, Sb, Pb, Pa, lp(Q), lp(P) + 49152, lp(Q) + 8192, lp(P) + 57344, 0, sQ + 49152, dvo, srdKV, sk + 16384u);
                P ^= 65536u; Q ^= 65536u; sP ^= 65536u; sQ ^= 65536u; sk += 65536u;
                if constexpr (HI) WAITV(8);
                if constexpr (!HI) { if (it == 1 && wid == 0) { waitv<0>(pv); poll_finish(pv, ctl, vo16 >> 1, (unsigned)(WS_FKV + h * 512), (vo16 >> 7) <= (unsigned)((15 - j) >> 1), tag); } }
                asm volatile("s_barrier" : "+v"(P), "+v"(Q)::"memory");
            }
            t = 2 * npair;
        }
        const unsigned B0 = lds0 + vo16;
#pragma unroll 1
        for (; t < tl; ++t) {
            const lds_cptr c = lp(B0 + (t & 3) * 32768), n = lp(B0 + ((t + 1) & 3) * 32768), p = lp(B0 + ((t - 1) & 3) * 32768);
            half_step<HI, false, true>(s, Sb, Sa, Pa, Pb, c + 8192, p + 24576, n, c + 16384, 0, rfl(m0b + ((t + 3) & 3) * 32768), dvo, srdKV, (unsigned)(t + 3) * 32768u);
            half_step<HI, true, true>(s, Sa, Sb, Pb, Pa, n, c + 16384, n + 8192, c + 24576, row - (64 * t + 64) - 4 * hi, rfl(m0b + ((t + 2) & 3) * 32768 + 16384), dvo, srdKV, (unsigned)(t + 2) * 32768u + 16384u);
            STEP_END();
        }
        {
            const lds_cptr c = lp(B0 + (t & 3) * 32768), p = lp(B0 + ((t - 1) & 3) * 32768);
            half_step<HI, true, true>(s, Sb, Sa, Pa, Pb, c + 8192, p + 24576, c, c + 16384, row - (64 * t + 32) - 4 * hi, rfl(m0b + ((t + 3) & 3) * 32768), dvo, srdKV, (unsigned)(t + 3) * 32768u);
            half_step<HI, false, false>(s, Sa, Sb, Pb, Pa, c, c + 16384, c + 24576, c, 0, rfl(m0b + ((t + 2) & 3) * 32768 + 16384), dvo, srdKV, (unsigned)(t + 2) * 32768u + 16384u);
            s.fr[7] = lds_rd128(c + 24576 + 7 * 1024); s.fr[6] = lds_rd128(c + 24576 + 6 * 1024);
#pragma unroll
            for (int i = 0; i < 8; ++i) s.o[i & 3] = MFMA(s.fr[i], __builtin_bit_cast(bf16x8, Pb[i >> 2]), s.o[i & 3]);
            asm volatile("s_waitcnt lgkmcnt(0)" ::: "memory");
            STEP_END();
            ++t;
        }
#undef STEP_END
        STAMPL(8);
        {
            auto rr = __builtin_amdgcn_permlane32_swap(__float_as_uint(s.l), __float_as_uint(s.l), false, false);
            const float inv = __builtin_amdgcn_rcpf(__uint_as_float(rr[0]) + __uint_as_float(rr[1]));
            const srd_t srdC = mk_srd(ws + WS_CTX + (size_t)(h * 8) * KSTR + qg * 1024, 8 * KSTR);
#pragma unroll
            for (int d = 0; d < 4; ++d)
#pragma unroll
                for (int q = 0; q < 2; ++q) st16_wt(pack8(s.o[d], q, inv), srdC, vo16, (unsigned)(d * 2 + q) * KSTR);
        }
        if constexpr (HI) {
#define KVDMA() do { dma16x4(rfl(m0b + ((t + 3) & 3) * 32768), dvo, srdKV, (unsigned)(t + 3) * 32768u); \
                     dma16x4(rfl(m0b + ((t + 2) & 3) * 32768 + 16384), dvo, srdKV, (unsigned)(t + 2) * 32768u + 16384u); } while (0)
            KVDMA(); WAITV(16); BARRIER(); ++t;
            u32x4 wsink = {0, 0, 0, 0};
            KVDMA(); WAITV(8);
            flag_set(ctl, pcf, tag); cpub = true;
            { const unsigned gw = bx * 4 + w4; warm<4>(wsink, A.wo, 4096 * DM * 2, gw, vo16); warm<2>(wsink, A.wd, FF * DM * 2, gw, vo16); }
            if (t + 1 == NT) WAITV(0);
            BARRIER(); ++t;
            if (t < NT) { KVDMA(); if (t + 1 == NT) WAITV(0); else WAITV(15); BARRIER(); ++t; }
#pragma unroll 1
            for (; t < NT; ++t) { KVDMA(); if (t + 1 == NT) WAITV(0); else WAITV(8); BARRIER(); }
            PIN(wsink);
#undef KVDMA
        } else {
            if (t < NT) { WAITV(0); flag_set(ctl, pcf, tag); cpub = true; BARRIER(); }
        }
    }
    STAMPL(9);
    {
        const int lane = lane_id(), r32 = lane & 31, hi = lane >> 5, trq = (lane & 15) >> 2, trp = lane & 3, trb = (lane >> 4) & 1;
        const int rt = xcd * 4 + (bi >> 3), ct = bi & 7;
        const int f0 = wid * 11 - (wid > 6 ? 1 : 0), f1 = f0 + (wid < 6 ? 11 : 10);
        const srd_t srdH = mk_srd(ws + WS_H + rt * 2048 + (size_t)f0 * KSTR, (unsigned)(f1 - f0) * KSTR - rt * 2048);
        const srd_t srdD = mk_srd(A.wd + (size_t)f0 * 16 * DM + ct * 64, (unsigned)(f1 - f0) * 16384u - ct * 128);
        const srd_t srdC = mk_srd(ws + WS_CTX + rt * 2048 + (size_t)wid * 32 * KSTR, 32u * KSTR - rt * 2048);
        const srd_t srdO = mk_srd(A.wo + (size_t)wid * 512 * DM + ct * 64, 32u * 16384u - ct * 128);
        const unsigned voP = (unsigned)(((lane >> 2) & 7) * 1024 + (lane >> 5) * 64 + (lane & 3) * 16);
        const unsigned m0w = lds0 + wid * 16384;
        unsigned rp = lds0 + (unsigned)(wid * 16384 + (4 * hi + trq) * 64 + trb * 32 + trp * 8);
        PIN(rp);
        f32x16 af[2][2], as[2][2];
#pragma unroll
        for (int i = 0; i < 4; ++i) { af[i >> 1][i & 1] = f32x16{}; as[i >> 1][i & 1] = f32x16{}; }
        bf16x8 bf[8][2];
        s16x4 wt[2][4];
#define IS_W(slot, isf, k, half) dma16(m0w + (slot) * 2048 + (half) * 1024, voP, (isf) ? srdD : srdO, (unsigned)(k) * 16384u + (half) * 8192u)
#define IS_B(slot, isf, k) do { bufld16<0>(bf[slot][0], vo16, (isf) ? srdH : srdC, (unsigned)(k) * KSTR); bufld16<1024>(bf[slot][1], vo16, (isf) ? srdH : srdC, (unsigned)(k) * KSTR); } while (0)
#define RD_W(buf, slot) do { wt[buf][0] = lds_tr(lp(rp) + (slot) * 2048); wt[buf][1] = lds_tr(lp(rp) + (slot) * 2048 + 1024); \
                             wt[buf][2] = lds_tr(lp(rp) + (slot) * 2048 + 512); wt[buf][3] = lds_tr(lp(rp) + (slot) * 2048 + 1536); } while (0)
#define STEP(acc, slot, buf, WAITN, nisf, nk, DOISSUE, DONEXT) do { \
        if (DONEXT) { asm volatile("s_waitcnt vmcnt(" #WAITN ")" : "+v"(bf[((slot) + 1) & 7][0]), "+v"(bf[((slot) + 1) & 7][1])::"memory"); RD_W((buf) ^ 1, ((slot) + 1) & 7); SBAR(); } \
        const bf16x8 w0_ = cat8(wt[buf][0], wt[buf][1]), w1_ = cat8(wt[buf][2], wt[buf][3]); \
        acc[0][0] = MFMA(w0_, bf[slot][0], acc[0][0]); SBAR(); \
        acc[1][0] = MFMA(w1_, bf[slot][0], acc[1][0]); SBAR(); \
        if (DOISSUE) { IS_W(slot, nisf, nk, 0); SBAR(); } \
        acc[0][1] = MFMA(w0_, bf[slot][1], acc[0][1]); SBAR(); \
        if (DOISSUE) { IS_W(slot, nisf, nk, 1); SBAR(); } \
        acc[1][1] = MFMA(w1_, bf[slot][1], acc[1][1]); asm volatile("" : "+v"(acc[0][0]), "+v"(acc[0][1]), "+v"(acc[1][0]), "+v"(acc[1][1])); SBAR(); \
        if (DOISSUE) { IS_B(slot, nisf, nk); SBAR(); } } while (0)
        u32x2 ph, pc, xr[2];
        const unsigned phf = (unsigned)(WS_FH + rt * 512), pcc = (unsigned)(WS_FC + rt * 512), vo8 = vo16 >> 1;
        poll_issue(ph, ctl, vo8, phf);
        asm volatile("s_nop 4");
#pragma unroll
        for (int i = 0; i < 8; ++i) { IS_W(i, true, i, 0); IS_W(i, true, i, 1); }
        waitv<16>(ph);
        if (!cpub) flag_set(ctl, pcf, tag);
        poll_finish(ph, ctl, vo8, phf, true, tag);
#pragma unroll
        for (int i = 0; i < 8; ++i) IS_B(i, true, i);
        poll_issue(pc, ctl, vo8, pcc);
        const int un = wid * 2;
#pragma unroll
        for (int uu = 0; uu < 2; ++uu) {
            const int unit = un + uu, blk = unit >> 2, g = unit & 3, nb = blk >> 1, q = blk & 1;
            asm volatile("global_load_dwordx2 %0, %1, off" : "=v"(xr[uu]) : "v"(A.x + (size_t)(rt * 64 + q * 32 + r32) * DM + ct * 64 + nb * 32 + g * 8 + 4 * hi) : "memory");
        }
        asm volatile("s_waitcnt vmcnt(17)" : "+v"(bf[0][0]), "+v"(bf[0][1])::"memory");
        RD_W(0, 0); SBAR();
        STEP(af, 0, 0, 15, true, 8, true, true);  STEP(af, 1, 1, 17, true, 9, true, true);  STEP(af, 2, 0, 19, true, 10, true, true);
        waitv<14>(pc);
        poll_finish(pc, ctl, vo8, pcc, true, tag);
        SBAR();
        STEP(af, 3, 1, 21, false, 0, true, true); STEP(af, 4, 0, 23, false, 1, true, true); STEP(af, 5, 1, 24, false, 2, true, true);
        STEP(af, 6, 0, 24, false, 3, true, true); STEP(af, 7, 1, 24, false, 4, true, true); STEP(af, 0, 0, 24, false, 5, true, true);
        STEP(af, 1, 1, 24, false, 6, true, true); STEP(af, 2, 0, 24, false, 7, true, true);
        STAMPL(10);
#pragma unroll 1
        for (int i = 0; i < 24; i += 8) {
            STEP(as, 3, 1, 24, false, i + 8, true, true);  STEP(as, 4, 0, 24, false, i + 9, true, true);  STEP(as, 5, 1, 24, false, i + 10, true, true);
            STEP(as, 6, 0, 24, false, i + 11, true, true); STEP(as, 7, 1, 24, false, i + 12, true, true); STEP(as, 0, 0, 24, false, i + 13, true, true);
            STEP(as, 1, 1, 24, false, i + 14, true, true); STEP(as, 2, 0, 24, false, i + 15, true, true);
        }
        STEP(as, 3, 1, 24, false, 0, false, true); STEP(as, 4, 0, 20, false, 0, false, true); STEP(as, 5, 1, 16, false, 0, false, true);
        STEP(as, 6, 0, 12, false, 0, false, true); STEP(as, 7, 1, 8, false, 0, false, true);  STEP(as, 0, 0, 4, false, 0, false, true);
        STEP(as, 1, 1, 0, false, 0, false, true);  STEP(as, 2, 0, 0, false, 0, false, false);
#undef STEP
#undef RD_W
#undef IS_B
#undef IS_W
        asm volatile("" : "+v"(xr[0]), "+v"(xr[1]));
        STAMPL(11);
        f32x4 red[2][2];
#pragma unroll
        for (int pass = 0; pass < 2; ++pass) {
#pragma unroll
            for (int nb = 0; nb < 2; ++nb)
#pragma unroll
                for (int q = 0; q < 2; ++q)
#pragma unroll
                    for (int g = 0; g < 4; ++g) {
                        const f32x16& a = pass ? as[nb][q] : af[nb][q];
                        const f32x4 v = {a[4 * g], a[4 * g + 1], a[4 * g + 2], a[4 * g + 3]};
                        *(LAS f32x4*)(lds + wid * 16384 + (((nb * 2 + q) * 4 + g) * 64 + lane) * 16) = v;
                    }
            asm volatile("s_waitcnt lgkmcnt(0)\n\ts_barrier" ::: "memory");
#pragma unroll
            for (int uu = 0; uu < 2; ++uu) {
                f32x4 sv = {0.f, 0.f, 0.f, 0.f};
#pragma unroll
                for (int w = 0; w < 8; ++w) sv += *(const LAS f32x4*)(lds + w * 16384 + ((un + uu) * 64 + lane) * 16);
                red[pass][uu] = sv;
            }
            if (pass == 0) asm volatile("s_waitcnt lgkmcnt(0)\n\ts_barrier" ::: "memory");
        }
#pragma unroll
        for (int uu = 0; uu < 2; ++uu) {
            const int unit = un + uu, blk = unit >> 2, g = unit & 3, nb = blk >> 1, q = blk & 1;
            const int row = rt * 64 + q * 32 + r32, col = ct * 64 + nb * 32 + g * 8 + 4 * hi;
            const float xv[4] = {__uint_as_float(xr[uu][0] << 16), __uint_as_float(xr[uu][0] & 0xffff0000u), __uint_as_float(xr[uu][1] << 16), __uint_as_float(xr[uu][1] & 0xffff0000u)};
            float ov[4];
#pragma unroll
            for (int e = 0; e < 4; ++e) {
                const float f = red[0][uu][e];
                const float sg = f * __builtin_amdgcn_rcpf(1.f + __builtin_amdgcn_exp2f(-1.4426950409f * f));
                ov[e] = xv[e] + red[1][uu][e] * sg;
            }
            u32x2 w; w[0] = cvtpk(ov[0], ov[1]); w[1] = cvtpk(ov[2], ov[3]);
            *(u32x2*)(A.out + (size_t)row * DM + col) = w;
        }
        if (rt == 31 && ct == 7 && wid == 0) flag_set(ctl, (unsigned)WS_SEED, tag);
    }
    STAMPL(12); STAMPR(15);
}

__global__ __launch_bounds__(512, 2) void k_fused(Args A) {
    extern __shared__ __attribute__((aligned(16))) char lds_raw[];
    const int wid = rfl(threadIdx.x >> 6);
    if (wid >= 4) run<true>(A, (lds_ptr)lds_raw, wid);
    else run<false>(A, (lds_ptr)lds_raw, wid);
}

extern "C" void kernel_launch(void* const* d_in, const int* in_sizes, int n_in, void* d_out, int out_size, void* d_ws, size_t ws_size,
                              hipStream_t stream) {
    Args A;
    A.x = (const u16*)d_in[0]; A.ckv = (const u16*)d_in[1]; A.wq = (const u16*)d_in[2]; A.wuk = (const u16*)d_in[3]; A.wuv = (const u16*)d_in[4];
    A.wo = (const u16*)d_in[5]; A.wg = (const u16*)d_in[7]; A.wu = (const u16*)d_in[8]; A.wd = (const u16*)d_in[9];
    A.out = (u16*)d_out; A.ws = (char*)d_ws;
    (void)hipFuncSetAttribute((const void*)k_fused, hipFuncAttributeMaxDynamicSharedMemorySize, LDS_BYTES);
    k_fused<<<256, 512, LDS_BYTES, stream>>>(A);
}
```

```cpp
#include <hip/hip_runtime.h>
#include <stdint.h>
#include <utility>

typedef short bf16x8 __attribute__((ext_vector_type(8)));
typedef short s16x4 __attribute__((ext_vector_type(4)));
typedef float f32x16 __attribute__((ext_vector_type(16)));
typedef float f32x4 __attribute__((ext_vector_type(4)));
typedef float f32x2 __attribute__((ext_vector_type(2)));
typedef __bf16 bf16x2_t __attribute__((ext_vector_type(2)));
typedef unsigned u32x4 __attribute__((ext_vector_type(4)));
typedef unsigned u32x2 __attribute__((ext_vector_type(2)));
typedef unsigned short u16;
typedef unsigned long long u64;
#define LAS __attribute__((address_space(3)))
#define GAS __attribute__((address_space(1)))
typedef LAS const char* lds_cptr;
typedef LAS char* lds_ptr;
typedef __amdgpu_buffer_rsrc_t srd_t;
#define DI __device__ __forceinline__
#define SBAR() __builtin_amdgcn_sched_barrier(0)
#define PIN(x) asm volatile("" : "+v"(x))
#define MFMA(a, b, c) __builtin_amdgcn_mfma_f32_32x32x16_bf16((a), (b), (c), 0, 0, 0)
template <int N> using IC = std::integral_constant<int, N>;

#define STAMPL(i) do {} while (0)
#define STAMPR(i) do {} while (0)
constexpr int DM = 512, HD = 128, FF = 1376;
constexpr size_t MiB = 1u << 20;
constexpr unsigned KSTR = 65 * 1024;
constexpr size_t WS_SEED = 0, WS_FX = 4096, WS_FKV = 8192, WS_FH = 24576, WS_FC = 40960;
constexpr size_t WS_XN = 1 * MiB, WS_KV = 4 * MiB, WS_CTX = 36 * MiB, WS_H = WS_CTX + (size_t)256 * KSTR, WS_WF = 58 * MiB, WS_TS = 64 * MiB;
constexpr unsigned WSTR = 17 * 1024;
constexpr int LDS_BYTES = 163840, L_WA = 0, L_WB = 106496;
constexpr float QSCALE = 0.08838834764831845f * 1.4426950408889634f;
constexpr float THR = 8.f;

DI unsigned rfl(unsigned x) { return __builtin_amdgcn_readfirstlane(x); }
DI unsigned cvtpk(float lo, float hi) { f32x2 v = {lo, hi}; bf16x2_t b = __builtin_convertvector(v, bf16x2_t); return __builtin_bit_cast(unsigned, b); }
DI bf16x8 lds_rd128(lds_cptr p) { return *(const LAS bf16x8*)p; }
typedef short v4i16_t __attribute__((ext_vector_type(4)));
DI s16x4 lds_tr(lds_cptr p) { return __builtin_bit_cast(s16x4, __builtin_amdgcn_ds_read_tr16_b64_v4i16((LAS v4i16_t*)p)); }
DI bf16x8 cat8(s16x4 a, s16x4 b) { return __builtin_shufflevector(a, b, 0, 1, 2, 3, 4, 5, 6, 7); }
DI lds_cptr lp(unsigned a) { return (lds_cptr)(uintptr_t)a; }
DI srd_t mk_srd(const void* p, unsigned bytes) { return __builtin_amdgcn_make_buffer_rsrc((void*)p, (short)0, (int)bytes, 0x00020000); }
DI void dma16(unsigned m0v, unsigned voff, srd_t srd, unsigned soff) {
    asm volatile("s_mov_b32 m0, %0\n\ts_nop 0\n\tbuffer_load_dwordx4 %1, %2, %3 offen lds" ::"s"(m0v), "v"(voff), "s"(srd), "s"(soff) : "memory");
}
DI void dma16x4(unsigned m0v, unsigned voff, srd_t srd, unsigned soff) {
    asm volatile("s_mov_b32 m0, %0\n\ts_nop 0\n\tbuffer_load_dwordx4 %1, %2, %3 offen sc1 lds\n\tbuffer_load_dwordx4 %1, %2, %3 offen offset:1024 sc1 lds\n\t"
                 "buffer_load_dwordx4 %1, %2, %3 offen offset:2048 sc1 lds\n\tbuffer_load_dwordx4 %1, %2, %3 offen offset:3072 sc1 lds"
                 ::"s"(m0v), "v"(voff), "s"(srd), "s"(soff) : "memory");
}
DI void dma16x2(unsigned m0v, unsigned voff, srd_t srd, unsigned soff) {
    asm volatile("s_mov_b32 m0, %0\n\ts_nop 0\n\tbuffer_load_dwordx4 %1, %2, %3 offen sc1 lds\n\tbuffer_load_dwordx4 %1, %2, %3 offen offset:1024 sc1 lds"
                 ::"s"(m0v), "v"(voff), "s"(srd), "s"(soff) : "memory");
}
template <int OFF> DI void bufld16(bf16x8& dst, unsigned voff, srd_t srd, unsigned soff) {
    asm volatile("buffer_load_dwordx4 %0, %1, %2, %3 offen offset:%4 sc1" : "=v"(dst) : "v"(voff), "s"(srd), "s"(soff), "i"(OFF) : "memory");
}
DI void st16_wt(u32x4 v, srd_t srd, unsigned voff, unsigned soff) {
    asm volatile("buffer_store_dwordx4 %0, %1, %2, %3 offen sc1\n\ts_nop 1" ::"v"(v), "v"(voff), "s"(srd), "s"(soff) : "memory");
}
template <int N> DI void warm(u32x4& dst, const void* base, unsigned bytes, unsigned gw, unsigned vo16) {
    const srd_t srd = mk_srd(base, bytes);
#pragma unroll
    for (int i = 0; i < N; ++i)
        asm volatile("buffer_load_dwordx4 %0, %1, %2, %3 offen sc1" : "+v"(dst) : "v"(vo16), "s"(srd), "s"((gw * N + i) * 1024u) : "memory");
}
#define WAITV(n) asm volatile("s_waitcnt vmcnt(" #n ")" ::: "memory")
template <int N, class T> DI void waitv(T& x) { asm volatile("s_waitcnt vmcnt(%1)" : "+v"(x) : "n"(N) : "memory"); }
#define BARRIER() asm volatile("s_barrier" ::: "memory")
DI u32x4 pack8(const f32x16& x, int s, float sc) {
    u32x4 w;
    w[0] = cvtpk(x[8 * s + 0] * sc, x[8 * s + 1] * sc); w[1] = cvtpk(x[8 * s + 2] * sc, x[8 * s + 3] * sc);
    w[2] = cvtpk(x[8 * s + 4] * sc, x[8 * s + 5] * sc); w[3] = cvtpk(x[8 * s + 6] * sc, x[8 * s + 7] * sc);
    return w;
}
DI u32x4 pack8n(const f32x16& x, int s) {
    u32x4 w;
    w[0] = cvtpk(x[8 * s + 0], x[8 * s + 1]); w[1] = cvtpk(x[8 * s + 2], x[8 * s + 3]);
    w[2] = cvtpk(x[8 * s + 4], x[8 * s + 5]); w[3] = cvtpk(x[8 * s + 6], x[8 * s + 7]);
    return w;
}
template <int... Is, class F> DI void static_for_(std::integer_sequence<int, Is...>, F&& f) { (f(IC<Is>{}), ...); }
template <int N, class F> DI void static_for(F&& f) { static_for_(std::make_integer_sequence<int, N>{}, f); }

DI void flag_set(srd_t ctl, unsigned soff, u64 tag) {
    const u32x2 v = {(unsigned)tag, (unsigned)(tag >> 32)};
    asm volatile("buffer_store_dwordx2 %0, off, %1, %2 sc1\n\ts_nop 0" ::"v"(v), "s"(ctl), "s"(soff) : "memory");
}
DI void poll_issue(u32x2& v, srd_t ctl, unsigned voff, unsigned soff) { asm volatile("buffer_load_dwordx2 %0, %1, %2, %3 offen sc1" : "=v"(v) : "v"(voff), "s"(ctl), "s"(soff) : "memory"); }
DI void poll_finish(u32x2 v, srd_t ctl, unsigned voff, unsigned soff, bool need, u64 tag) {
    if (__builtin_expect(__builtin_amdgcn_ballot_w64(need && (((u64)v[1] << 32) | v[0]) != tag) != 0, 0)) {
        const u64 t0 = __builtin_amdgcn_s_memrealtime();
        for (;;) {
            __builtin_amdgcn_s_sleep(1);
            asm volatile("buffer_load_dwordx2 %0, %1, %2, %3 offen sc1\n\ts_waitcnt vmcnt(0)" : "=&v"(v) : "v"(voff), "s"(ctl), "s"(soff) : "memory");
            if (__builtin_amdgcn_ballot_w64(need && (((u64)v[1] << 32) | v[0]) != tag) == 0) break;
            if (__builtin_amdgcn_s_memrealtime() - t0 > 400000ull) break;
        }
    }
    asm volatile("" ::: "memory");
}
DI int lane_id() { int l; asm volatile("v_mbcnt_lo_u32_b32 %0, -1, 0\n\tv_mbcnt_hi_u32_b32 %0, -1, %0" : "=v"(l)); return l; }

#define MX3(a, b, c) __builtin_fmaxf(__builtin_fmaxf((a), (b)), (c))
#define EX(v) __builtin_amdgcn_exp2f(v)
struct AttSt {
    f32x16 o[4]; bf16x8 qf[8]; bf16x8 fr[8]; f32x16 negm;
    float l, m;
};
DI void cmask(f32x16& p, int dq) {
#pragma unroll
    for (int r = 0; r < 16; ++r) { const int c = (r & 3) + 8 * (r >> 2); p[r] = (c > dq) ? -__builtin_inff() : p[r]; }
}
template <bool DMA, bool MASK, bool DOQK>
DI void half_step(AttSt& s, f32x16& Sn, f32x16& Sc, u32x4 (&Pc)[2], u32x4 (&Pp)[2], lds_cptr kp, lds_cptr vp, lds_cptr np, lds_cptr vc, int dq,
                  unsigned dm0, unsigned dvo, srd_t dsrd, unsigned dso) {
    float r0, r1, ma, mb;
#define RD1(n) s.fr[(n) & 7] = lds_rd128(((n) < 8 ? kp : (n) < 16 ? vp : np) + ((n) & 7) * 1024)
#define RD(i) do { if constexpr (DOQK || (i) + 6 >= 8) { RD1((i) + 7); RD1((i) + 6); } } while (0)
#define QK(i) do { if constexpr (DOQK) Sn = MFMA(s.fr[i], s.qf[i], (i) == 0 ? s.negm : Sn); } while (0)
#define PV(i) s.o[(i) & 3] = MFMA(s.fr[i], __builtin_bit_cast(bf16x8, Pp[(i) >> 2]), s.o[(i) & 3])
#define E(i) Sc[i] = EX(Sc[i])
#define C(i) Pc[(i) >> 2][(i) & 3] = cvtpk(Sc[2 * (i)], Sc[2 * (i) + 1])
#define ENDG() do { PIN(Sc); SBAR(); } while (0)
    SBAR();
    QK(0); RD(0); E(0); ENDG();
    QK(1); E(1); E(2); E(3); ENDG();
    QK(2); RD(2); E(4); C(0); PIN(Pc[0]); ENDG();
    QK(3); E(5); E(6); r0 = Sc[0] + Sc[2]; r1 = Sc[1] + Sc[3]; PIN(r0); PIN(r1); ENDG();
    QK(4); RD(4); E(7); C(1); PIN(Pc[0]); ENDG();
    QK(5); E(8); r0 += Sc[4]; r1 += Sc[5]; C(2); r0 += Sc[6]; PIN(r0); PIN(r1); PIN(Pc[0]); ENDG();
    QK(6); RD(6); E(9); r1 += Sc[7]; PIN(r1); ENDG();
    QK(7); E(10); E(11); C(3); r0 += Sc[8]; PIN(r0); PIN(Pc[0]); ENDG();
    PV(0); RD(8); E(12); r1 += Sc[9]; PIN(r1); ENDG();
    if constexpr (DMA) { dma16x4(dm0, dvo, dsrd, dso); SBAR(); }
    PV(1); E(13); C(4); r0 += Sc[10]; r1 += Sc[11]; C(5); PIN(r0); PIN(r1); PIN(Pc[1]); ENDG();
    if constexpr (MASK) { cmask(Sn, dq); SBAR(); }
    PV(2); RD(10); E(14); if constexpr (DOQK) { ma = MX3(Sn[0], Sn[1], Sn[2]); PIN(ma); } ENDG();
    PV(3); E(15); r0 += Sc[12]; r1 += Sc[13]; C(6); if constexpr (DOQK) { mb = MX3(Sn[3], Sn[4], Sn[5]); PIN(mb); } PIN(r0); PIN(r1); PIN(Pc[1]); ENDG();
    PV(4); RD(12); r0 += Sc[14]; if constexpr (DOQK) { ma = MX3(ma, Sn[6], Sn[7]); mb = MX3(mb, Sn[8], Sn[9]); PIN(ma); PIN(mb); } PIN(r0); ENDG();
    PV(5); r1 += Sc[15]; C(7); if constexpr (DOQK) { ma = MX3(ma, Sn[10], Sn[11]); mb = MX3(mb, Sn[12], Sn[13]); ma = MX3(ma, Sn[14], Sn[15]); PIN(ma); PIN(mb); } PIN(r1); PIN(Pc[1]); SBAR();
    PV(6); RD(14); r0 += r1; s.l += r0; PIN(s.l); SBAR();
    PV(7);
    if constexpr (DOQK) {
        ma = MX3(ma, mb, Sn[15]);
        if (__builtin_expect(__builtin_amdgcn_ballot_w64(ma > THR) != 0, 0)) {
            auto rr = __builtin_amdgcn_permlane32_swap(__float_as_uint(ma), __float_as_uint(ma), false, false);
            const float dl = __builtin_fmaxf(__builtin_fmaxf(__uint_as_float(rr[0]), __uint_as_float(rr[1])), 0.f);
#pragma unroll
            for (int i = 0; i < 8; ++i) { const bf16x8 f = lds_rd128(vc + i * 1024); s.o[i & 3] = MFMA(f, __builtin_bit_cast(bf16x8, Pc[i >> 2]), s.o[i & 3]); }
            const float f = EX(-dl);
            s.m += dl; s.l *= f;
#pragma unroll
            for (int d = 0; d < 4; ++d)
#pragma unroll
                for (int r = 0; r < 16; ++r) s.o[d][r] *= f;
#pragma unroll
            for (int r = 0; r < 16; ++r) { Sn[r] -= dl; s.negm[r] = -s.m; }
            Pc[0] = u32x4{0, 0, 0, 0}; Pc[1] = u32x4{0, 0, 0, 0};
            PIN(s.negm); PIN(Sn); PIN(Pc[0]); PIN(Pc[1]);
        }
    }
    SBAR();
#undef RD1
#undef RD
#undef QK
#undef PV
#undef E
#undef C
#undef ENDG
}

struct Args { const u16 *x, *ckv, *wq, *wuk, *wuv, *wo, *wg, *wu, *wd; u16* out; char* ws; };

template <bool HI>
DI void run(const Args& A, lds_ptr lds, const int wid) {
    STAMPL(0); STAMPR(14);
    const unsigned lds0 = (unsigned)(uintptr_t)lds;
    const int lane = threadIdx.x & 63, r32 = lane & 31, hi = lane >> 5, w4 = wid & 3;
    const int bx = blockIdx.x, xcd = bx & 7, bi = bx >> 3;
    const int h = xcd + 8 * (bi >> 3), j = bi & 7;
    const int trq = (lane & 15) >> 2, trp = lane & 3, trb = (lane >> 4) & 1;
    const unsigned vo16 = (unsigned)(lane * 16);
    char* const ws = A.ws;
    const u64 seed = *(const u64*)(ws + WS_SEED);
    const u64 tag = seed + 1;
    const srd_t ctl = mk_srd(ws, 65536);
    const int qb = HI ? j : 15 - j, qg = qb * 4 + w4;
    const int NT = 2 * (15 - j) + 2;
    const srd_t srdKV = mk_srd(ws + WS_KV + (size_t)h * 32 * 32768, (unsigned)NT * 32768u);
    const int u0 = h < 22 ? 3 * h : 66 + 2 * (h - 22), nu = h < 22 ? 3 : 2;
    const srd_t srdW = mk_srd(A.wq + h * HD, DM * 4096 * 2 - h * HD * 2);
    const srd_t srdF = mk_srd((HI ? A.wu : A.wg) + u0 * 16, DM * FF * 2 - u0 * 32);
    const srd_t srdX = mk_srd(ws + WS_XN + qg * 1024, 32 * KSTR - qg * 1024);
    const unsigned voW = (unsigned)((((lane >> 2) & 7) + wid * 16) * 8192 + (lane >> 5) * 64 + (lane & 3) * 16);
    const unsigned m0W = lds0 + wid * 4096, m0F = lds0 + 32768 + wid * 3072;
    bf16x8 xf[8];
    s16x4 wl[5], wh[5];
    u32x4 kvp[16];
    unsigned voF[3];
    auto dmaW = [&](int c, int slot, auto ic) {
        constexpr int i = decltype(ic)::value;
        if constexpr (i < 4) dma16(m0W + slot + i * 1024, voW, srdW, (unsigned)((c * 128 + (i >> 1) * 8) * 8192 + (i & 1) * 128));
        else if constexpr (i < 7) dma16(m0F + slot + (i - 4) * 1024, voF[i - 4], srdF, (unsigned)(c * 128 * FF * 2));
    };
    auto ldX = [&](int c, auto kc) { constexpr int k = decltype(kc)::value; bufld16<0>(xf[k], vo16, srdX, (unsigned)(c * 8 + k) * KSTR); };
    unsigned bW, bF0, bF1;
    auto rdW = [&](auto mc) {
        constexpr int m = decltype(mc)::value, ks = (m / 7) & 7, b = m % 7, e = m % 5;
        if constexpr (b < 4) { wl[e] = lds_tr(lp(bW) + ks * 4096 + b * 512); wh[e] = lds_tr(lp(bW) + ks * 4096 + b * 512 + 256); }
        else { wl[e] = lds_tr(lp(bF0) + ks * 1536 + (b - 4) * 32); wh[e] = lds_tr(lp(bF1) + ks * 1536 + (b - 4) * 32); }
    };

    {
        const int wr = wid & 1, wc1 = (wid >> 1) & 1;
        const srd_t srdA = mk_srd(A.ckv + (size_t)j * 256 * DM, 256 * DM * 2);
        const srd_t srdB = mk_srd((HI ? A.wuv : A.wuk) + h * HD, DM * 4096 * 2 - h * HD * 2);
        unsigned voA[2];
#pragma unroll
        for (int par = 0; par < 2; ++par) voA[par] = (unsigned)((32 * wid + (lane >> 3)) * 1024 + (((lane & 7) ^ (4 * par + (lane >> 4))) * 16));
        const unsigned voB = (unsigned)((((lane >> 2) & 7) + w4 * 16) * 8192 + (lane >> 5) * 64 + (lane & 3) * 16);
        const unsigned m0A = lds0 + wid * 4096, m0B = lds0 + 32768 + wid * 4096;
        auto dma = [&](int kt0, int slot, auto ic) {
            constexpr int i = decltype(ic)::value;
            const int kt = (kt0 + xcd) & 7;
            if constexpr (i < 4) dma16(m0A + slot * 65536 + i * 1024, voA[i & 1], srdA, (unsigned)(kt * 128 + i * 8192));
            else dma16(m0B + slot * 65536 + (i - 4) * 1024, voB, srdB, (unsigned)(kt * 64 * 8192 + ((i - 4) >> 1) * 8 * 8192 + (i & 1) * 128));
        };
        const int row = bx * 8 + wid;
        u32x4 raw;
        asm volatile("s_nop 4\n\tglobal_load_dwordx4 %0, %1, %2" : "=v"(raw) : "v"(vo16), "s"(A.x + (size_t)row * DM) : "memory");
        static_for<8>([&](auto ic) { dma(0, 0, ic); });
        BARRIER();
        static_for<8>([&](auto ic) { dma(1, 1, ic); });
        const int tks = row >> 2;
        const srd_t srdT = mk_srd((tks < 256 ? A.wo + (size_t)tks * 16 * DM : A.wd + (size_t)(tks - 256) * 16 * DM) + (row & 3) * 128, tks < 342 ? 16 * DM * 2 - (row & 3) * 256 : 0);
        const srd_t srdTo = mk_srd(ws + WS_WF + (size_t)tks * WSTR + (row & 3) * 4096, tks < 342 ? 4096 : 0);
        { const unsigned voT = (unsigned)(((lane >> 2) & 7) * 1024 + (lane >> 5) * 64 + (lane & 3) * 16);
#pragma unroll
          for (int i = 0; i < 4; ++i) dma16(lds0 + 131072 + wid * 4096 + i * 1024, voT, srdT, (unsigned)((i >> 1) * 8192 + (i & 1) * 128)); }
        {
            waitv<20>(raw);
            float v[8];
#pragma unroll
            for (int i = 0; i < 4; ++i) { v[2 * i] = __uint_as_float(raw[i] << 16); v[2 * i + 1] = __uint_as_float(raw[i] & 0xffff0000u); }
            float s = 0.f;
#pragma unroll
            for (int i = 0; i < 8; ++i) s += v[i];
#pragma unroll
            for (int o = 1; o < 64; o <<= 1) s += __shfl_xor(s, o);
            const float mean = s * (1.f / DM);
            float q = 0.f;
#pragma unroll
            for (int i = 0; i < 8; ++i) { v[i] -= mean; q += v[i] * v[i]; }
#pragma unroll
            for (int o = 1; o < 64; o <<= 1) q += __shfl_xor(q, o);
            const float rstd = 1.f / sqrtf(q * (1.f / DM) + 1e-5f);
            u32x4 w;
#pragma unroll
            for (int i = 0; i < 4; ++i) w[i] = cvtpk(v[2 * i] * rstd, v[2 * i + 1] * rstd);
            st16_wt(w, mk_srd(ws + WS_XN, 32 * KSTR), (unsigned)((lane >> 1) * KSTR + (lane & 1) * 512), (unsigned)((row >> 5) * 1024 + (row & 31) * 16));
        }
        unsigned aoff[4];
#pragma unroll
        for (int ks = 0; ks < 4; ++ks) { aoff[ks] = lds0 + (unsigned)((wr * 128 + r32) * 128 + (((2 * ks + hi) ^ ((r32 >> 1) & 7)) * 16)); PIN(aoff[ks]); }
        unsigned boff = lds0 + (unsigned)(32768 + (HI ? 16384 : 0) + wc1 * 1024 + hi * 2048 + trq * 64 + trb * 32 + trp * 8);
        PIN(boff);
        f32x16 acc[4][2];
#pragma unroll
        for (int a = 0; a < 4; ++a)
#pragma unroll
            for (int b = 0; b < 2; ++b) acc[a][b] = f32x16{};
        bf16x8 cf[2][4]; s16x4 kl[2][2], kh[2][2];
        auto rd = [&](int buf, int ks, auto ic) {
            constexpr int i = decltype(ic)::value;
            if constexpr (i == 0) kl[buf][0] = lds_tr(lp(boff) + ks * 4096);
            if constexpr (i == 1) kh[buf][0] = lds_tr(lp(boff) + ks * 4096 + 256);
            if constexpr (i == 2) cf[buf][0] = lds_rd128(lp(aoff[ks]));
            if constexpr (i == 3) kl[buf][1] = lds_tr(lp(boff) + ks * 4096 + 512);
            if constexpr (i == 4) kh[buf][1] = lds_tr(lp(boff) + ks * 4096 + 768);
            if constexpr (i >= 5) cf[buf][i - 4] = lds_rd128(lp(aoff[ks]) + (i - 4) * 4096);
        };
        auto mm = [&](int buf, auto ic) {
            constexpr int i = decltype(ic)::value;
            const bf16x8 w_ = cat8(kl[buf][i & 1], kh[buf][i & 1]);
            acc[i >> 1][i & 1] = HI ? MFMA(cf[buf][i >> 1], w_, acc[i >> 1][i & 1]) : MFMA(w_, cf[buf][i >> 1], acc[i >> 1][i & 1]);
        };
        WAITV(13); BARRIER();
        STAMPL(1);
        static_for<8>([&](auto ic) { rd(0, 0, ic); });
        SBAR();
#define KV_HEAD(WN) do { \
        static_for<8>([&](auto ic) { mm(0, ic); rd(1, 1, ic); SBAR(); }); \
        static_for<8>([&](auto ic) { mm(1, ic); rd(0, 2, ic); SBAR(); }); \
        static_for<8>([&](auto ic) { mm(0, ic); rd(1, 3, ic); SBAR(); }); \
        boff ^= 65536u; aoff[0] ^= 65536u; aoff[1] ^= 65536u; aoff[2] ^= 65536u; aoff[3] ^= 65536u; \
        asm volatile("s_waitcnt vmcnt(" #WN ") lgkmcnt(0)\n\ts_barrier" : "+v"(boff), "+v"(aoff[0]), "+v"(aoff[1]), "+v"(aoff[2]), "+v"(aoff[3])::"memory"); SBAR(); } while (0)
#define KV_TAIL(kt) static_for<8>([&](auto ic) { mm(1, ic); rd(0, 0, ic); dma((kt) + 2, (kt) & 1, ic); SBAR(); })
        u32x4 wsink = {0, 0, 0, 0};
        KV_HEAD(0);
        KV_TAIL(0);
        {
            const lds_cptr tp = lp(lds0 + 131072 + wid * 4096 + (4 * hi + trq) * 64 + trb * 32 + trp * 8);
#pragma unroll
            for (int nb = 0; nb < 4; ++nb) st16_wt(__builtin_bit_cast(u32x4, cat8(lds_tr(tp + nb * 512), lds_tr(tp + 2048 + nb * 512))), srdTo, vo16, (unsigned)(nb * 1024));
            warm<2>(wsink, A.wq, DM * 4096 * 2, row, vo16); warm<1>(wsink, A.wg, DM * FF * 2, row, vo16); warm<1>(wsink, A.wu, DM * FF * 2, row, vo16);
        }
        KV_HEAD(4);
        if (wid == 0) flag_set(ctl, (unsigned)(WS_FX + bx * 8), tag);
        KV_TAIL(1);
        KV_HEAD(0); PIN(wsink); KV_TAIL(2);
#pragma unroll 1
        for (int kt = 3; kt < 5; ++kt) { KV_HEAD(0); KV_TAIL(kt); }
        u32x4 pv0, pv1;
        if constexpr (!HI) { if (wid == 0) asm volatile("buffer_load_dwordx4 %0, %2, %3, %4 offen sc1\n\tbuffer_load_dwordx4 %1, %2, %3, %4 offen offset:1024 sc1" : "=&v"(pv0), "=&v"(pv1) : "v"(vo16), "s"(ctl), "s"((unsigned)WS_FX) : "memory"); }
        KV_HEAD(0);
        if constexpr (!HI) { if (wid == 0) { PIN(pv0); PIN(pv1);
            poll_finish(u32x2{pv0[0], pv0[1]}, ctl, vo16, (unsigned)WS_FX, true, tag); poll_finish(u32x2{pv0[2], pv0[3]}, ctl, vo16, (unsigned)WS_FX + 8, true, tag);
            poll_finish(u32x2{pv1[0], pv1[1]}, ctl, vo16, (unsigned)WS_FX + 1024, true, tag); poll_finish(u32x2{pv1[2], pv1[3]}, ctl, vo16, (unsigned)WS_FX + 1032, true, tag); } }
        KV_TAIL(5);
#pragma unroll
        for (int i = 0; i < 3; ++i) { const int g = (3 * w4 + i) * 64 + lane, r = g / 6, c = g - 6 * r; voF[i] = (unsigned)((HI ? (r ^ 4) : r) * (FF * 2) + c * 16); }
        KV_HEAD(0);
        static_for<8>([&](auto ic) { mm(1, ic); rd(0, 0, ic); dmaW(0, L_WA, ic); ldX(0, ic); SBAR(); });
        bW = lds0 + L_WA + (unsigned)(hi * 2048 + trq * 64 + trb * 32 + trp * 8);
        bF0 = lds0 + L_WA + (unsigned)(32768 + trb * 12288 + hi * 768 + (trq + 4 * trb) * 96 + trp * 8);
        bF1 = lds0 + L_WA + (unsigned)(32768 + trb * 12288 + hi * 768 + (trq + 4 - 4 * trb) * 96 + trp * 8);
        PIN(bW); PIN(bF0); PIN(bF1);
        static_for<8>([&](auto ic) { mm(0, ic); rd(1, 1, ic); SBAR(); });
        static_for<8>([&](auto ic) { mm(1, ic); rd(0, 2, ic); SBAR(); });
        static_for<8>([&](auto ic) { mm(0, ic); rd(1, 3, ic); SBAR(); });
        asm volatile("s_waitcnt vmcnt(0) lgkmcnt(0)\n\ts_barrier" : "+v"(xf[0]), "+v"(xf[1]), "+v"(xf[2]), "+v"(xf[3]), "+v"(xf[4]), "+v"(xf[5]), "+v"(xf[6]), "+v"(xf[7])::"memory");
        SBAR();
        static_for<8>([&](auto ic) { constexpr int i = decltype(ic)::value; mm(1, ic); if constexpr (i < 4) rdW(ic); dmaW(1, L_WB, ic); SBAR(); });
#undef KV_HEAD
#undef KV_TAIL
#pragma unroll
        for (int i = 0; i < 16; ++i) { kvp[i] = pack8n(acc[i >> 2][(i >> 1) & 1], i & 1); PIN(kvp[i]); }
        STAMPL(2);
    }
    AttSt s;
    {
        const int wr = wid & 1, wc1 = (wid >> 1) & 1;
        const srd_t srdO = mk_srd(ws + WS_KV + (size_t)h * 32 * 32768 + (HI ? 16384 : 0), 32 * 32768);
        auto stKV = [&](auto ic) {
            constexpr int i = decltype(ic)::value, kb = i >> 2, db = (i >> 1) & 1, sh = i & 1;
            const int u = j * 8 + wr * 4 + kb, dblk = 2 * wc1 + db, fi = HI ? (sh * 4 + dblk) : (2 * dblk + sh);
            st16_wt(kvp[i], srdO, vo16, (unsigned)((u >> 1) * 32768 + (u & 1) * 8192 + fi * 1024));
        };
        const int smode = j >> 1;
        const unsigned pkv = (unsigned)(WS_FKV + (h * 64 + j * 8 + wid) * 8);
        f32x16 acc[7];
#pragma unroll
        for (int d = 0; d < 7; ++d) acc[d] = f32x16{};
        u32x2 pv;
        static_for<224>([&](auto mc) {
            constexpr int m = decltype(mc)::value, c = m / 56, k = (m / 7) & 7, b = m % 7, r = m % 56;
            if constexpr (b == 0 && c > 0) { constexpr int wn = c < 3 ? (k < 7 ? 14 : 7) : (k == 0 ? 7 : 17 - k); waitv<wn>(xf[k]); SBAR(); }
            if constexpr (r == 52 && c < 3) {
                constexpr unsigned dl = (c & 1) ? (unsigned)(L_WA - L_WB) : (unsigned)(L_WB - L_WA);
                bW += dl; bF0 += dl; bF1 += dl;
                asm volatile("s_waitcnt vmcnt(%3) lgkmcnt(0)\n\ts_barrier" : "+v"(bW), "+v"(bF0), "+v"(bF1) : "n"(c == 0 ? 7 : 8) : "memory");
                SBAR();
                STAMPL(3 + c);
            }
            acc[b] = MFMA(cat8(wl[m % 5], wh[m % 5]), xf[k], acc[b]);
            if constexpr (m + 4 < 224) rdW(IC<m + 4>{});
            if constexpr (r >= 52 && c < 2) { dmaW(c + 2, (c & 1) ? L_WB : L_WA, IC<2 * (r - 52)>{}); dmaW(c + 2, (c & 1) ? L_WB : L_WA, IC<2 * (r - 52) + 1>{}); }
            if constexpr (c == 3 && r < 2) dma16x4(lds0 + wid * 10240 + r * 4096, vo16, srdKV, (unsigned)(wid * 10240 + r * 4096));
            if constexpr (c == 3 && r == 2) dma16x2(lds0 + wid * 10240 + 8192, vo16, srdKV, (unsigned)(wid * 10240 + 8192));
            if constexpr (b == 6) {
                PIN(acc[0]); PIN(acc[1]); PIN(acc[2]); PIN(acc[3]); PIN(acc[4]); PIN(acc[5]); PIN(acc[6]);
                if (smode == c) { stKV(IC<2 * k>{}); stKV(IC<2 * k + 1>{}); }
                if constexpr (c < 3) ldX(c + 1, IC<k>{});
                if constexpr (c > 0 && k == 4) { if (smode == c - 1) { if constexpr (c < 3) WAITV(6); else WAITV(11); flag_set(ctl, pkv, tag); } }
                if constexpr (!HI && c == 2 && k == 1) { if (wid == 0) poll_issue(pv, ctl, vo16 >> 1, (unsigned)(WS_FKV + h * 512)); }
                if constexpr (!HI && c == 2 && k == 4) { if (wid == 0) { waitv<3>(pv); poll_finish(pv, ctl, vo16 >> 1, (unsigned)(WS_FKV + h * 512), vo16 < 256, tag); } }
            }
            SBAR();
        });
        STAMPL(6);
#pragma unroll
        for (int d = 0; d < 4; ++d)
#pragma unroll
            for (int q = 0; q < 2; ++q) s.qf[2 * d + q] = __builtin_bit_cast(bf16x8, pack8(acc[d], q, QSCALE));
        u32x4 hw[3];
#pragma unroll
        for (int v = 0; v < 3; ++v) {
            float hv[8];
#pragma unroll
            for (int r = 0; r < 8; ++r) {
                const float g = acc[4 + v][r];
                const float z = g * (1.5957691216f + 0.0713548163f * g * g);
                const float e = __builtin_amdgcn_exp2f(-1.4426950409f * z);
                hv[r] = g * __builtin_amdgcn_rcpf(1.f + e) * acc[4 + v][8 + r];
            }
            hw[v] = u32x4{cvtpk(hv[0], hv[1]), cvtpk(hv[2], hv[3]), cvtpk(hv[4], hv[5]), cvtpk(hv[6], hv[7])};
        }
        asm volatile("s_waitcnt vmcnt(0) lgkmcnt(0)\n\ts_barrier" : "+v"(hw[0]), "+v"(hw[1]), "+v"(hw[2])::"memory");
        if (smode == 3) flag_set(ctl, pkv, tag);
        const srd_t srdH = mk_srd(ws + WS_H + (size_t)u0 * KSTR + qg * 1024, (unsigned)nu * KSTR - qg * 1024);
#pragma unroll
        for (int v = 0; v < 3; ++v) st16_wt(hw[v], srdH, vo16, (unsigned)v * KSTR);
    }
    STAMPL(7);
    bool cpub = false;
    const unsigned pcf = (unsigned)(WS_FC + (qg * 32 + h) * 8);
    {
        const int lane = lane_id(), r32 = lane & 31, hi = lane >> 5;
        const int tl = qg >> 1;
        const int row = qg * 32 + r32;
        const unsigned dvo = (unsigned)(lane * 16 + w4 * 4096);
        const unsigned m0b = lds0 + w4 * 4096;
        f32x16 Sa, Sb; u32x4 Pa[2], Pb[2];
        Pb[0] = u32x4{0, 0, 0, 0}; Pb[1] = u32x4{0, 0, 0, 0}; Pa[0] = Pb[0]; Pa[1] = Pb[0];
        unsigned P = lds0 + vo16, Q = lds0 + 65536 + vo16;
        PIN(P); PIN(Q);
        {
#pragma unroll
            for (int i = 0; i < 8; ++i) s.fr[i] = lds_rd128(lp(P) + i * 1024);
            SBAR();
            f32x16 z = f32x16{};
            static_for<8>([&](auto ic) { constexpr int i = decltype(ic)::value; z = MFMA(s.fr[i], s.qf[i], z);
                if constexpr (i == 1 || i == 3 || i == 5) { s.fr[i] = lds_rd128(lp(P) + 8192 + i * 1024); s.fr[i - 1] = lds_rd128(lp(P) + 8192 + (i - 1) * 1024); } SBAR(); });
            Sa = z;
            if (tl == 0) cmask(Sa, row - 4 * hi);
            float ma = MX3(Sa[0], Sa[1], Sa[2]), mb = MX3(Sa[3], Sa[4], Sa[5]);
            ma = MX3(ma, Sa[6], Sa[7]); mb = MX3(mb, Sa[8], Sa[9]); ma = MX3(ma, Sa[10], Sa[11]); mb = MX3(mb, Sa[12], Sa[13]); ma = MX3(ma, Sa[14], Sa[15]);
            ma = __builtin_fmaxf(ma, mb);
            auto rr = __builtin_amdgcn_permlane32_swap(__float_as_uint(ma), __float_as_uint(ma), false, false);
            const float rm = __builtin_fmaxf(__uint_as_float(rr[0]), __uint_as_float(rr[1]));
            s.m = rm; s.l = 0.f;
#pragma unroll
            for (int r = 0; r < 16; ++r) { Sa[r] -= rm; s.negm[r] = -rm; }
#pragma unroll
            for (int d = 0; d < 4; ++d) s.o[d] = f32x16{};
            PIN(s.negm);
        }
        bool hpub = false;
        const unsigned phf = (unsigned)(WS_FH + (qg * 32 + h) * 8);
#define STEP_END() do { if constexpr (HI) WAITV(8); if (!hpub) { if constexpr (!HI) WAITV(0); flag_set(ctl, phf, tag); hpub = true; } BARRIER(); } while (0)
        int t = 0;
        const int npair = (tl - 1) >> 1;
        if (npair > 0) {
            unsigned sP = m0b, sQ = m0b + 65536, sk = 3 * 32768u;
#pragma unroll 1
            for (int it = 0; it < npair; ++it) {
                half_step<HI, false, true>(s, Sb, Sa, Pa, Pb, lp(P) + 8192, lp(Q) + 57344, lp(P) + 32768, lp(P) + 16384, 0, sQ + 32768, dvo, srdKV, sk);
                half_step<HI, false, true>(s, Sa, Sb, Pb, Pa, lp(P) + 32768, lp(P) + 16384, lp(P) + 40960, lp(P) + 24576, 0, sQ + 16384, dvo, srdKV, sk - 16384u);
                u32x2 pv;
                if constexpr (!HI) { if (it == 1 && wid == 0) poll_issue(pv, ctl, vo16 >> 1, (unsigned)(WS_FKV + h * 512)); }
                STEP_END();
                half_step<HI, false, true>(s, Sb, Sa, Pa, Pb, lp(P) + 40960, lp(P) + 24576, lp(Q), lp(P) + 49152, 0, sP, dvo, srdKV, sk + 32768u);
                half_step<HI, false, true>(s, Sa, Sb, Pb, Pa, lp(Q), lp(P) + 49152, lp(Q) + 8192, lp(P) + 57344, 0, sQ + 49152, dvo, srdKV, sk + 16384u);
                P ^= 65536u; Q ^= 65536u; sP ^= 65536u; sQ ^= 65536u; sk += 65536u;
                if constexpr (HI) WAITV(8);
                if constexpr (!HI) { if (it == 1 && wid == 0) { waitv<0>(pv); poll_finish(pv, ctl, vo16 >> 1, (unsigned)(WS_FKV + h * 512), (vo16 >> 7) <= (unsigned)((15 - j) >> 1), tag); } }
                asm volatile("s_barrier" : "+v"(P), "+v"(Q)::"memory");
            }
            t = 2 * npair;
        }
        const unsigned B0 = lds0 + vo16;
#pragma unroll 1
        for (; t < tl; ++t) {
            const lds_cptr c = lp(B0 + (t & 3) * 32768), n = lp(B0 + ((t + 1) & 3) * 32768), p = lp(B0 + ((t - 1) & 3) * 32768);
            half_step<HI, false, true>(s, Sb, Sa, Pa, Pb, c + 8192, p + 24576, n, c + 16384, 0, rfl(m0b + ((t + 3) & 3) * 32768), dvo, srdKV, (unsigned)(t + 3) * 32768u);
            half_step<HI, true, true>(s, Sa, Sb, Pb, Pa, n, c + 16384, n + 8192, c + 24576, row - (64 * t + 64) - 4 * hi, rfl(m0b + ((t + 2) & 3) * 32768 + 16384), dvo, srdKV, (unsigned)(t + 2) * 32768u + 16384u);
            STEP_END();
        }
        {
            const lds_cptr c = lp(B0 + (t & 3) * 32768), p = lp(B0 + ((t - 1) & 3) * 32768);
            half_step<HI, true, true>(s, Sb, Sa, Pa, Pb, c + 8192, p + 24576, c, c + 16384, row - (64 * t + 32) - 4 * hi, rfl(m0b + ((t + 3) & 3) * 32768), dvo, srdKV, (unsigned)(t + 3) * 32768u);
            half_step<HI, false, false>(s, Sa, Sb, Pb, Pa, c, c + 16384, c + 24576, c, 0, rfl(m0b + ((t + 2) & 3) * 32768 + 16384), dvo, srdKV, (unsigned)(t + 2) * 32768u + 16384u);
            s.fr[7] = lds_rd128(c + 24576 + 7 * 1024); s.fr[6] = lds_rd128(c + 24576 + 6 * 1024);
#pragma unroll
            for (int i = 0; i < 8; ++i) s.o[i & 3] = MFMA(s.fr[i], __builtin_bit_cast(bf16x8, Pb[i >> 2]), s.o[i & 3]);
            asm volatile("s_waitcnt lgkmcnt(0)" ::: "memory");
            STEP_END();
            ++t;
        }
#undef STEP_END
        STAMPL(8);
        {
            auto rr = __builtin_amdgcn_permlane32_swap(__float_as_uint(s.l), __float_as_uint(s.l), false, false);
            const float inv = __builtin_amdgcn_rcpf(__uint_as_float(rr[0]) + __uint_as_float(rr[1]));
            const srd_t srdC = mk_srd(ws + WS_CTX + (size_t)(h * 8) * KSTR + qg * 1024, 8 * KSTR);
#pragma unroll
            for (int d = 0; d < 4; ++d)
#pragma unroll
                for (int q = 0; q < 2; ++q) st16_wt(pack8(s.o[d], q, inv), srdC, vo16, (unsigned)(d * 2 + q) * KSTR);
        }
        if constexpr (HI) {
#define KVDMA() do { dma16x4(rfl(m0b + ((t + 3) & 3) * 32768), dvo, srdKV, (unsigned)(t + 3) * 32768u); \
                     dma16x4(rfl(m0b + ((t + 2) & 3) * 32768 + 16384), dvo, srdKV, (unsigned)(t + 2) * 32768u + 16384u); } while (0)
            KVDMA(); WAITV(16); BARRIER(); ++t;
            KVDMA(); if (t + 1 == NT) WAITV(0); else WAITV(8);
            flag_set(ctl, pcf, tag); cpub = true;
            BARRIER(); ++t;
#pragma unroll 1
            for (; t < NT; ++t) { KVDMA(); if (t + 1 == NT) WAITV(0); else WAITV(8); BARRIER(); }
#undef KVDMA
        } else {
            if (t < NT) { WAITV(0); flag_set(ctl, pcf, tag); cpub = true; BARRIER(); }
        }
    }
    STAMPL(9);
    {
        const int lane = lane_id(), r32 = lane & 31, hi = lane >> 5;
        const int rt = xcd * 4 + (bi >> 3), ct = bi & 7;
        const int f0 = wid * 11 - (wid > 6 ? 1 : 0), f1 = f0 + (wid < 6 ? 11 : 10);
        const srd_t srdH = mk_srd(ws + WS_H + rt * 2048 + (size_t)f0 * KSTR, (unsigned)(f1 - f0) * KSTR - rt * 2048);
        const srd_t srdD = mk_srd(ws + WS_WF + ct * 2048 + (size_t)(256 + f0) * WSTR, (unsigned)(f1 - f0) * WSTR - ct * 2048);
        const srd_t srdC = mk_srd(ws + WS_CTX + rt * 2048 + (size_t)wid * 32 * KSTR, 32u * KSTR - rt * 2048);
        const srd_t srdO = mk_srd(ws + WS_WF + ct * 2048 + (size_t)wid * 32 * WSTR, 32u * WSTR - ct * 2048);
        f32x16 acc[2][2];
#pragma unroll
        for (int i = 0; i < 4; ++i) acc[i >> 1][i & 1] = f32x16{};
        bf16x8 bf[8][2], wf[8][2];
#define IS_W(slot, isf, k) do { bufld16<0>(wf[slot][0], vo16, (isf) ? srdD : srdO, (unsigned)(k) * WSTR); bufld16<1024>(wf[slot][1], vo16, (isf) ? srdD : srdO, (unsigned)(k) * WSTR); } while (0)
#define IS_B(slot, isf, k) do { bufld16<0>(bf[slot][0], vo16, (isf) ? srdH : srdC, (unsigned)(k) * KSTR); bufld16<1024>(bf[slot][1], vo16, (isf) ? srdH : srdC, (unsigned)(k) * KSTR); } while (0)
#define STEP(slot, WAITN, nisf, nk, DOISSUE) do { \
        asm volatile("s_waitcnt vmcnt(" #WAITN ")" : "+v"(bf[slot][0]), "+v"(bf[slot][1]), "+v"(wf[slot][0]), "+v"(wf[slot][1])::"memory"); \
        acc[0][0] = MFMA(wf[slot][0], bf[slot][0], acc[0][0]); acc[1][0] = MFMA(wf[slot][1], bf[slot][0], acc[1][0]); \
        acc[0][1] = MFMA(wf[slot][0], bf[slot][1], acc[0][1]); acc[1][1] = MFMA(wf[slot][1], bf[slot][1], acc[1][1]); \
        asm volatile("" : "+v"(acc[0][0]), "+v"(acc[0][1]), "+v"(acc[1][0]), "+v"(acc[1][1])); SBAR(); \
        if (DOISSUE) { IS_W(slot, nisf, nk); IS_B(slot, nisf, nk); SBAR(); } } while (0)
#define PUT_PARTIAL() do { _Pragma("unroll") for (int i_ = 0; i_ < 16; ++i_) { const f32x16& a_ = acc[i_ >> 3][(i_ >> 2) & 1]; const int g_ = i_ & 3; \
            *(LAS f32x4*)(lds + wid * 16384 + i_ * 1024 + vo16) = f32x4{a_[4 * g_], a_[4 * g_ + 1], a_[4 * g_ + 2], a_[4 * g_ + 3]}; } } while (0)
#define GET_SUMS(dst) do { _Pragma("unroll") for (int uu = 0; uu < 2; ++uu) { f32x4 sv = {0.f, 0.f, 0.f, 0.f}; \
            _Pragma("unroll") for (int w = 0; w < 8; ++w) sv += *(const LAS f32x4*)(lds + w * 16384 + (un + uu) * 1024 + vo16); dst[uu] = sv; } } while (0)
        u32x2 ph, pc, xr[2];
        const unsigned phf = (unsigned)(WS_FH + rt * 512), pcc = (unsigned)(WS_FC + rt * 512), vo8 = vo16 >> 1;
        poll_issue(ph, ctl, vo8, phf);
#pragma unroll
        for (int i = 0; i < 8; ++i) IS_W(i, true, i);
        waitv<16>(ph);
        if (!cpub) flag_set(ctl, pcf, tag);
        poll_finish(ph, ctl, vo8, phf, true, tag);
#pragma unroll
        for (int i = 0; i < 8; ++i) IS_B(i, true, i);
        poll_issue(pc, ctl, vo8, pcc);
        const int un = wid * 2;
#pragma unroll
        for (int uu = 0; uu < 2; ++uu) {
            const int unit = un + uu, blk = unit >> 2, g = unit & 3, nb = blk >> 1, q = blk & 1;
            asm volatile("global_load_dwordx2 %0, %1, off" : "=v"(xr[uu]) : "v"(A.x + (size_t)(rt * 64 + q * 32 + r32) * DM + ct * 64 + nb * 32 + g * 8 + 4 * hi) : "memory");
        }
        STEP(0, 17, true, 8, true);  STEP(1, 19, true, 9, true);  STEP(2, 21, true, 10, true);
        waitv<14>(pc);
        poll_finish(pc, ctl, vo8, pcc, true, tag);
        SBAR();
        STEP(3, 23, false, 0, true); STEP(4, 25, false, 1, true); STEP(5, 27, false, 2, true); STEP(6, 28, false, 3, true);
        STEP(7, 28, false, 4, true); STEP(0, 28, false, 5, true); STEP(1, 28, false, 6, true); STEP(2, 28, false, 7, true);
        PUT_PARTIAL();
#pragma unroll
        for (int i = 0; i < 4; ++i) acc[i >> 1][i & 1] = f32x16{};
        SBAR();
        STAMPL(10);
#pragma unroll 1
        for (int i = 0; i < 24; i += 8) {
            STEP(3, 28, false, i + 8, true);  STEP(4, 28, false, i + 9, true);  STEP(5, 28, false, i + 10, true); STEP(6, 28, false, i + 11, true);
            STEP(7, 28, false, i + 12, true); STEP(0, 28, false, i + 13, true); STEP(1, 28, false, i + 14, true); STEP(2, 28, false, i + 15, true);
        }
        STEP(3, 28, false, 0, false); STEP(4, 24, false, 0, false); STEP(5, 20, false, 0, false); STEP(6, 16, false, 0, false);
        STEP(7, 12, false, 0, false); STEP(0, 8, false, 0, false);  STEP(1, 4, false, 0, false);  STEP(2, 0, false, 0, false);
#undef STEP
#undef IS_B
#undef IS_W
        asm volatile("" : "+v"(xr[0]), "+v"(xr[1]));
        STAMPL(11);
        f32x4 red[2][2];
        asm volatile("s_waitcnt lgkmcnt(0)\n\ts_barrier" ::: "memory");
        GET_SUMS(red[0]);
        asm volatile("s_waitcnt lgkmcnt(0)\n\ts_barrier" : "+v"(red[0][0]), "+v"(red[0][1])::"memory");
        PUT_PARTIAL();
        asm volatile("s_waitcnt lgkmcnt(0)\n\ts_barrier" ::: "memory");
        GET_SUMS(red[1]);
#undef PUT_PARTIAL
#undef GET_SUMS
#pragma unroll
        for (int uu = 0; uu < 2; ++uu) {
            const int unit = un + uu, blk = unit >> 2, g = unit & 3, nb = blk >> 1, q = blk & 1;
            const int row = rt * 64 + q * 32 + r32, col = ct * 64 + nb * 32 + g * 8 + 4 * hi;
            const float xv[4] = {__uint_as_float(xr[uu][0] << 16), __uint_as_float(xr[uu][0] & 0xffff0000u), __uint_as_float(xr[uu][1] << 16), __uint_as_float(xr[uu][1] & 0xffff0000u)};
            float ov[4];
#pragma unroll
            for (int e = 0; e < 4; ++e) {
                const float f = red[0][uu][e];
                const float sg = f * __builtin_amdgcn_rcpf(1.f + __builtin_amdgcn_exp2f(-1.4426950409f * f));
                ov[e] = xv[e] + red[1][uu][e] * sg;
            }
            u32x2 w; w[0] = cvtpk(ov[0], ov[1]); w[1] = cvtpk(ov[2], ov[3]);
            *(u32x2*)(A.out + (size_t)row * DM + col) = w;
        }
        if (rt == 31 && ct == 7 && wid == 0) flag_set(ctl, (unsigned)WS_SEED, tag);
    }
    STAMPL(12); STAMPR(15);
}

__global__ __launch_bounds__(512, 2) void k_fused(Args A) {
    extern __shared__ __attribute__((aligned(16))) char lds_raw[];
    const int wid = rfl(threadIdx.x >> 6);
    if (wid >= 4) run<true>(A, (lds_ptr)lds_raw, wid);
    else run<false>(A, (lds_ptr)lds_raw, wid);
}

extern "C" void kernel_launch(void* const* d_in, const int* in_sizes, int n_in, void* d_out, int out_size, void* d_ws, size_t ws_size,
                              hipStream_t stream) {
    Args A;
    A.x = (const u16*)d_in[0]; A.ckv = (const u16*)d_in[1]; A.wq = (const u16*)d_in[2]; A.wuk = (const u16*)d_in[3]; A.wuv = (const u16*)d_in[4];
    A.wo = (const u16*)d_in[5]; A.wg = (const u16*)d_in[7]; A.wu = (const u16*)d_in[8]; A.wd = (const u16*)d_in[9];
    A.out = (u16*)d_out; A.ws = (char*)d_ws;
    (void)hipFuncSetAttribute((const void*)k_fused, hipFuncAttributeMaxDynamicSharedMemorySize, LDS_BYTES);
    k_fused<<<256, 512, LDS_BYTES, stream>>>(A);
}
```

```cpp
#include <hip/hip_runtime.h>
#include <stdint.h>
#include <utility>

typedef short bf16x8 __attribute__((ext_vector_type(8)));
typedef short s16x4 __attribute__((ext_vector_type(4)));
typedef float f32x16 __attribute__((ext_vector_type(16)));
typedef float f32x4 __attribute__((ext_vector_type(4)));
typedef float f32x2 __attribute__((ext_vector_type(2)));
typedef __bf16 bf16x2_t __attribute__((ext_vector_type(2)));
typedef unsigned u32x4 __attribute__((ext_vector_type(4)));
typedef unsigned u32x2 __attribute__((ext_vector_type(2)));
typedef unsigned short u16;
typedef unsigned long long u64;
#define LAS __attribute__((address_space(3)))
typedef LAS const char* lds_cptr;
typedef LAS char* lds_ptr;
typedef __amdgpu_buffer_rsrc_t srd_t;
#define DI __device__ __forceinline__
#define SBAR() __builtin_amdgcn_sched_barrier(0)
#define PIN(x) asm volatile("" : "+v"(x))
#define MFMA(a, b, c) __builtin_amdgcn_mfma_f32_32x32x16_bf16((a), (b), (c), 0, 0, 0)
template <int N> using IC = std::integral_constant<int, N>;

constexpr int PD = 4;
constexpr int WKT = 4;
constexpr int RDH = 9;
constexpr int DM = 512, HD = 128, FF = 1376;
constexpr size_t MiB = 1u << 20;
constexpr unsigned TSTR = 688 * 1024;
constexpr size_t WS_SEED = 0, WS_FX = 4096, WS_FKV = 8192, WS_FH = 24576, WS_FC = 40960, WS_FW = 57344;
constexpr size_t WS_XN = 2 * MiB, WS_KV = 4 * MiB, WS_CH = 36 * MiB, WS_WF = 58 * MiB;
constexpr int LDS_BYTES = 163840;
constexpr int L_QA = 131072, L_FA = 0, L_QB = 98304, L_FB = 49152;
constexpr float QSCALE = 0.08838834764831845f * 1.4426950408889634f;
constexpr float THR = 8.f;

DI void mfma16(f32x4& c, bf16x8 a, bf16x8 b) { asm volatile("v_mfma_f32_16x16x32_bf16 %0, %1, %2, %0" : "+v"(c) : "v"(a), "v"(b)); }
#define MFMA_DRAIN() asm volatile("s_nop 7\n\ts_nop 3" ::: "memory")
DI unsigned rfl(unsigned x) { return __builtin_amdgcn_readfirstlane(x); }
DI unsigned cvtpk(float lo, float hi) { f32x2 v = {lo, hi}; bf16x2_t b = __builtin_convertvector(v, bf16x2_t); return __builtin_bit_cast(unsigned, b); }
DI bf16x8 lds_rd128(lds_cptr p) { return *(const LAS bf16x8*)p; }
typedef short v4i16_t __attribute__((ext_vector_type(4)));
DI s16x4 lds_tr(lds_cptr p) { return __builtin_bit_cast(s16x4, __builtin_amdgcn_ds_read_tr16_b64_v4i16((LAS v4i16_t*)p)); }
DI bf16x8 cat8(s16x4 a, s16x4 b) { return __builtin_shufflevector(a, b, 0, 1, 2, 3, 4, 5, 6, 7); }
DI lds_cptr lp(unsigned a) { return (lds_cptr)(uintptr_t)a; }
DI srd_t mk_srd(const void* p, unsigned bytes) { return __builtin_amdgcn_make_buffer_rsrc((void*)p, (short)0, (int)bytes, 0x00020000); }
DI void dma16(unsigned m0v, unsigned voff, srd_t srd, unsigned soff) {
    asm volatile("s_mov_b32 m0, %0\n\ts_nop 0\n\tbuffer_load_dwordx4 %1, %2, %3 offen lds" ::"s"(m0v), "v"(voff), "s"(srd), "s"(soff) : "memory");
}
DI void dma16s(unsigned m0v, unsigned voff, srd_t srd, unsigned soff) {
    asm volatile("s_mov_b32 m0, %0\n\ts_nop 0\n\tbuffer_load_dwordx4 %1, %2, %3 offen sc1 lds" ::"s"(m0v), "v"(voff), "s"(srd), "s"(soff) : "memory");
}
DI void dma16x4(unsigned m0v, unsigned voff, srd_t srd, unsigned soff) {
    asm volatile("s_mov_b32 m0, %0\n\ts_nop 0\n\tbuffer_load_dwordx4 %1, %2, %3 offen sc1 lds\n\tbuffer_load_dwordx4 %1, %2, %3 offen offset:1024 sc1 lds\n\t"
                 "buffer_load_dwordx4 %1, %2, %3 offen offset:2048 sc1 lds\n\tbuffer_load_dwordx4 %1, %2, %3 offen offset:3072 sc1 lds"
                 ::"s"(m0v), "v"(voff), "s"(srd), "s"(soff) : "memory");
}
template <int OFF> DI void bufld16(bf16x8& dst, unsigned voff, srd_t srd, unsigned soff) {
    asm volatile("buffer_load_dwordx4 %0, %1, %2, %3 offen offset:%4 sc1" : "=v"(dst) : "v"(voff), "s"(srd), "s"(soff), "i"(OFF) : "memory");
}
DI void st16_wt(u32x4 v, srd_t srd, unsigned voff, unsigned soff) {
    asm volatile("buffer_store_dwordx4 %0, %1, %2, %3 offen sc1\n\ts_nop 1" ::"v"(v), "v"(voff), "s"(srd), "s"(soff) : "memory");
}
template <int N> DI void warm(u32x4& dst, const void* base, unsigned bytes, unsigned gw, unsigned vo16) {
    const srd_t srd = mk_srd(base, bytes);
#pragma unroll
    for (int i = 0; i < N; ++i)
        asm volatile("buffer_load_dwordx4 %0, %1, %2, %3 offen sc1" : "+v"(dst) : "v"(vo16), "s"(srd), "s"((gw * N + i) * 1024u) : "memory");
}
#define WAITV(n) asm volatile("s_waitcnt vmcnt(" #n ")" ::: "memory")
template <int N, class T> DI void waitv(T& x) { asm volatile("s_waitcnt vmcnt(%1)" : "+v"(x) : "n"(N) : "memory"); }
template <int N, class T> DI void wait4(T& a, T& b, T& c, T& d) { asm volatile("s_waitcnt vmcnt(%4)" : "+v"(a), "+v"(b), "+v"(c), "+v"(d) : "n"(N) : "memory"); }
#define BARRIER() asm volatile("s_barrier" ::: "memory")
DI u32x4 pack8(const f32x16& x, int s, float sc) {
    u32x4 w;
    w[0] = cvtpk(x[8 * s + 0] * sc, x[8 * s + 1] * sc); w[1] = cvtpk(x[8 * s + 2] * sc, x[8 * s + 3] * sc);
    w[2] = cvtpk(x[8 * s + 4] * sc, x[8 * s + 5] * sc); w[3] = cvtpk(x[8 * s + 6] * sc, x[8 * s + 7] * sc);
    return w;
}
DI u32x4 frag16(f32x4 a, f32x4 b) {
    unsigned a0 = cvtpk(a[0], a[1]), a1 = cvtpk(a[2], a[3]), b0 = cvtpk(b[0], b[1]), b1 = cvtpk(b[2], b[3]);
    auto r0 = __builtin_amdgcn_permlane16_swap(a0, b0, false, false); auto r1 = __builtin_amdgcn_permlane16_swap(a1, b1, false, false);
    auto q0 = __builtin_amdgcn_permlane32_swap(r0[0], r0[1], false, false); auto q1 = __builtin_amdgcn_permlane32_swap(r1[0], r1[1], false, false);
    return u32x4{q0[0], q1[0], q0[1], q1[1]};
}
template <int... Is, class F> DI void static_for_(std::integer_sequence<int, Is...>, F&& f) { (f(IC<Is>{}), ...); }
template <int N, class F> DI void static_for(F&& f) { static_for_(std::make_integer_sequence<int, N>{}, f); }

DI void flag_set(srd_t ctl, unsigned soff, u64 tag) {
    const u32x2 v = {(unsigned)tag, (unsigned)(tag >> 32)};
    asm volatile("buffer_store_dwordx2 %0, off, %1, %2 sc1\n\ts_nop 0" ::"v"(v), "s"(ctl), "s"(soff) : "memory");
}
DI void poll_issue(u32x2& v, srd_t ctl, unsigned voff, unsigned soff) { asm volatile("buffer_load_dwordx2 %0, %1, %2, %3 offen sc1" : "=v"(v) : "v"(voff), "s"(ctl), "s"(soff) : "memory"); }
DI void poll_finish(u32x2 v, srd_t ctl, unsigned voff, unsigned soff, bool need, u64 tag) {
    if (__builtin_expect(__builtin_amdgcn_ballot_w64(need && (((u64)v[1] << 32) | v[0]) != tag) != 0, 0)) {
        const u64 t0 = __builtin_amdgcn_s_memrealtime();
        for (;;) {
            __builtin_amdgcn_s_sleep(1);
            asm volatile("buffer_load_dwordx2 %0, %1, %2, %3 offen sc1\n\ts_waitcnt vmcnt(0)" : "=&v"(v) : "v"(voff), "s"(ctl), "s"(soff) : "memory");
            if (__builtin_amdgcn_ballot_w64(need && (((u64)v[1] << 32) | v[0]) != tag) == 0) break;
            if (__builtin_amdgcn_s_memrealtime() - t0 > 400000ull) break;
        }
    }
    asm volatile("" ::: "memory");
}
DI float wave_sum(float s) {
    s += __int_as_float(__builtin_amdgcn_update_dpp(0, __float_as_int(s), 0xB1, 0xf, 0xf, true));
    s += __int_as_float(__builtin_amdgcn_update_dpp(0, __float_as_int(s), 0x4E, 0xf, 0xf, true));
    s += __int_as_float(__builtin_amdgcn_update_dpp(0, __float_as_int(s), 0x141, 0xf, 0xf, true));
    s += __int_as_float(__builtin_amdgcn_update_dpp(0, __float_as_int(s), 0x140, 0xf, 0xf, true));
    auto a = __builtin_amdgcn_permlane16_swap(__float_as_uint(s), __float_as_uint(s), false, false);
    s = __uint_as_float(a[0]) + __uint_as_float(a[1]);
    auto b = __builtin_amdgcn_permlane32_swap(__float_as_uint(s), __float_as_uint(s), false, false);
    return __uint_as_float(b[0]) + __uint_as_float(b[1]);
}
DI int lane_id() { int l; asm volatile("v_mbcnt_lo_u32_b32 %0, -1, 0\n\tv_mbcnt_hi_u32_b32 %0, -1, %0" : "=v"(l)); return l; }

#define MX3(a, b, c) __builtin_fmaxf(__builtin_fmaxf((a), (b)), (c))
#define EX(v) __builtin_amdgcn_exp2f(v)
struct AttSt {
    f32x16 o[4]; bf16x8 qf[8]; bf16x8 fr[8]; f32x16 negm;
    float l, m;
};
DI void cmask(f32x16& p, int dq) {
#pragma unroll
    for (int r = 0; r < 16; ++r) { const int c = (r & 3) + 8 * (r >> 2); p[r] = (c > dq) ? -__builtin_inff() : p[r]; }
}
template <bool DMA, bool MASK, bool DOQK>
DI void half_step(AttSt& s, f32x16& Sn, f32x16& Sc, u32x4 (&Pc)[2], u32x4 (&Pp)[2], lds_cptr kp, lds_cptr vp, lds_cptr np, lds_cptr vc, int dq,
                  unsigned dm0, unsigned dvo, srd_t dsrd, unsigned dso) {
    float r0, r1, ma, mb;
#define RD1(n) s.fr[(n) & 7] = lds_rd128(((n) < 8 ? kp : (n) < 16 ? vp : np) + ((n) & 7) * 1024)
#define RD(i) do { if constexpr (DOQK || (i) + 6 >= 8) { RD1((i) + 7); RD1((i) + 6); } } while (0)
#define QK(i) do { if constexpr (DOQK) Sn = MFMA(s.fr[i], s.qf[i], (i) == 0 ? s.negm : Sn); } while (0)
#define PV(i) s.o[(i) & 3] = MFMA(s.fr[i], __builtin_bit_cast(bf16x8, Pp[(i) >> 2]), s.o[(i) & 3])
#define E(i) Sc[i] = EX(Sc[i])
#define C(i) Pc[(i) >> 2][(i) & 3] = cvtpk(Sc[2 * (i)], Sc[2 * (i) + 1])
#define ENDG() do { PIN(Sc); SBAR(); } while (0)
    SBAR();
    QK(0); RD(0); E(0); ENDG();
    QK(1); E(1); E(2); E(3); ENDG();
    QK(2); RD(2); E(4); C(0); PIN(Pc[0]); ENDG();
    QK(3); E(5); E(6); r0 = Sc[0] + Sc[2]; r1 = Sc[1] + Sc[3]; PIN(r0); PIN(r1); ENDG();
    QK(4); RD(4); E(7); C(1); PIN(Pc[0]); ENDG();
    QK(5); E(8); r0 += Sc[4]; r1 += Sc[5]; C(2); r0 += Sc[6]; PIN(r0); PIN(r1); PIN(Pc[0]); ENDG();
    QK(6); RD(6); E(9); r1 += Sc[7]; PIN(r1); ENDG();
    QK(7); E(10); E(11); C(3); r0 += Sc[8]; PIN(r0); PIN(Pc[0]); ENDG();
    PV(0); RD(8); E(12); r1 += Sc[9]; PIN(r1); ENDG();
    if constexpr (DMA) { dma16x4(dm0, dvo, dsrd, dso); SBAR(); }
    PV(1); E(13); C(4); r0 += Sc[10]; r1 += Sc[11]; C(5); PIN(r0); PIN(r1); PIN(Pc[1]); ENDG();
    if constexpr (MASK) { cmask(Sn, dq); SBAR(); }
    PV(2); RD(10); E(14); if constexpr (DOQK) { ma = MX3(Sn[0], Sn[1], Sn[2]); PIN(ma); } ENDG();
    PV(3); E(15); r0 += Sc[12]; r1 += Sc[13]; C(6); if constexpr (DOQK) { mb = MX3(Sn[3], Sn[4], Sn[5]); PIN(mb); } PIN(r0); PIN(r1); PIN(Pc[1]); ENDG();
    PV(4); RD(12); r0 += Sc[14]; if constexpr (DOQK) { ma = MX3(ma, Sn[6], Sn[7]); mb = MX3(mb, Sn[8], Sn[9]); PIN(ma); PIN(mb); } PIN(r0); ENDG();
    PV(5); r1 += Sc[15]; C(7); if constexpr (DOQK) { ma = MX3(ma, Sn[10], Sn[11]); mb = MX3(mb, Sn[12], Sn[13]); ma = MX3(ma, Sn[14], Sn[15]); PIN(ma); PIN(mb); } PIN(r1); PIN(Pc[1]); SBAR();
    u64 grew = 0;
    PV(6); if constexpr (DOQK) { ma = MX3(ma, mb, Sn[15]); PIN(ma); } RD(14);
    if constexpr (DOQK) asm volatile("v_cmp_lt_f32 %0, %1, %2" : "=s"(grew) : "s"(THR), "v"(ma));
    SBAR();
    PV(7); r0 += r1; s.l += r0; PIN(s.l); SBAR();
    if constexpr (DOQK) {
        if (__builtin_expect(grew != 0, 0)) {
            auto rr = __builtin_amdgcn_permlane32_swap(__float_as_uint(ma), __float_as_uint(ma), false, false);
            const float dl = __builtin_fmaxf(__builtin_fmaxf(__uint_as_float(rr[0]), __uint_as_float(rr[1])), 0.f);
#pragma unroll
            for (int i = 0; i < 8; ++i) { const bf16x8 f = lds_rd128(vc + i * 1024); s.o[i & 3] = MFMA(f, __builtin_bit_cast(bf16x8, Pc[i >> 2]), s.o[i & 3]); }
            const float f = EX(-dl);
            s.m += dl; s.l *= f;
#pragma unroll
            for (int d = 0; d < 4; ++d)
#pragma unroll
                for (int r = 0; r < 16; ++r) s.o[d][r] *= f;
#pragma unroll
            for (int r = 0; r < 16; ++r) { Sn[r] -= dl; s.negm[r] = -s.m; }
            Pc[0] = u32x4{0, 0, 0, 0}; Pc[1] = u32x4{0, 0, 0, 0};
            PIN(s.negm); PIN(Sn); PIN(Pc[0]); PIN(Pc[1]);
        }
    }
    SBAR();
#undef RD1
#undef RD
#undef QK
#undef PV
#undef E
#undef C
#undef ENDG
}

struct Args { const u16 *x, *ckv, *wq, *wuk, *wuv, *wo, *wg, *wu, *wd; u16* out; char* ws; };

template <bool HI>
DI void run(const Args& A, lds_ptr lds, const int wid) {
    const unsigned lds0 = (unsigned)(uintptr_t)lds;
    const int lane = threadIdx.x & 63, r32 = lane & 31, hi = lane >> 5, w4 = wid & 3;
    const int bx = blockIdx.x, xcd = bx & 7, bi = bx >> 3;
    const int h = xcd + 8 * (bi >> 3), j = bi & 7;
    const int trq = (lane & 15) >> 2, trp = lane & 3, trb = (lane >> 4) & 1;
    const unsigned vo16 = (unsigned)(lane * 16);
    char* const ws = A.ws;
    const srd_t ctl = mk_srd(ws, 65536);
    u32x2 seedv; u64 tag;
    asm volatile("s_nop 4\n\tbuffer_load_dwordx2 %0, off, %1, 0 sc1" : "=v"(seedv) : "s"(ctl) : "memory");
    const int qb = HI ? j : 15 - j, qg = qb * 4 + w4;
    const int NT = 2 * (15 - j) + 2;
    const srd_t srdKV = mk_srd(ws + WS_KV + (size_t)h * 32 * 32768, (unsigned)NT * 32768u);
    const int u0 = h < 22 ? 3 * h : 66 + 2 * (h - 22), nu = h < 22 ? 3 : 2;
    const srd_t srdW = mk_srd(A.wq + h * HD, DM * 4096 * 2 - h * HD * 2);
    const srd_t srdF = mk_srd((HI ? A.wu : A.wg) + u0 * 16, DM * FF * 2 - u0 * 32);
    const srd_t srdX = mk_srd(ws + WS_XN + qg * 32768, 32768);
    const int g4 = lane >> 4;
    const unsigned voX = (unsigned)((lane & 15) * 64 + g4 * 16);
    unsigned voW[2];
#pragma unroll
    for (int par = 0; par < 2; ++par) voW[par] = (unsigned)((((lane >> 2) & 7) + wid * 16) * 8192 + (lane >> 5) * 64 + ((lane & 3) ^ (2 * par)) * 16);
    const unsigned m0W = lds0 + wid * 4096, m0F = lds0 + wid * 3072;
    bf16x8 xf[8];
    s16x4 wl[PD + 1], wh[PD + 1];
    u32x4 kvp[16];
    unsigned voF[3];
    auto dmaW = [&](int c, auto ic) {
        constexpr int i = decltype(ic)::value;
        if constexpr (i < 4) dma16(m0W + ((c & 1) ? L_QB : L_QA) + i * 1024, voW[i >> 1], srdW, (unsigned)((c * 128 + (i >> 1) * 8) * 8192 + (i & 1) * 128));
        else if constexpr (i < 7) dma16(m0F + ((c & 1) ? L_FB : L_FA) + (i - 4) * 1024, voF[i - 4], srdF, (unsigned)(c * 128 * FF * 2));
    };
    auto ldX = [&](int c, auto ec) { constexpr int e = decltype(ec)::value; bufld16<(e & 3) * 1024>(xf[e], voX, srdX, (unsigned)(c * 8192 + (e >> 2) * 4096)); };
    unsigned bWe, bWo, bF0, bF1;
    auto rdW = [&](auto fc) {
        constexpr int f = decltype(fc)::value, ks = (f / 14) & 3, b = f % 14, e = f % (PD + 1);
        if constexpr (b < 8) { const lds_cptr p = lp((b & 1) ? bWo : bWe) + ks * 8192 + (b >> 1) * 512; wl[e] = lds_tr(p); wh[e] = lds_tr(p + 256); }
        else { constexpr int o = ((b - 8) & 1) * 12288 + ks * 3072 + ((b - 8) >> 1) * 32; wl[e] = lds_tr(lp(bF0) + o); wh[e] = lds_tr(lp(bF1) + o); }
    };

    {
        const int wr = wid & 1, wc1 = (wid >> 1) & 1;
        const srd_t srdA = mk_srd(A.ckv + (size_t)j * 256 * DM, 256 * DM * 2);
        const srd_t srdB = mk_srd((HI ? A.wuv - 64 * 4096 : A.wuk) + h * HD, (HI ? DM + 64 : DM) * 4096 * 2 - h * HD * 2);
        unsigned voA[2];
#pragma unroll
        for (int par = 0; par < 2; ++par) voA[par] = (unsigned)((32 * wid + (lane >> 3)) * 1024 + (((lane & 7) ^ (4 * par + (lane >> 4))) * 16));
        const unsigned m0A = lds0 + wid * 4096, m0B = lds0 + 65536 + wid * 4096;
        auto dmaA = [&](int kt0, int i) { const int kt = (kt0 + xcd) & 7; dma16(m0A + (kt0 & 1) * 32768 + i * 1024, voA[i & 1], srdA, (unsigned)(kt * 128 + i * 8192)); };
        auto dmaB = [&](int kt0, int i) { const int kt = (kt0 + xcd) & 7; dma16(m0B + (kt0 % 3) * 32768 + i * 1024, voW[i >> 1], srdB, (unsigned)(kt * 64 * 8192 + (i >> 1) * 8 * 8192 + (i & 1) * 128)); };
        const int row = bx * 8 + wid;
        u32x4 raw;
        asm volatile("s_nop 4\n\tglobal_load_dwordx4 %0, %1, %2" : "=v"(raw) : "v"(vo16), "s"(A.x + (size_t)row * DM) : "memory");
#pragma unroll
        for (int i = 0; i < 4; ++i) { dmaA(0, i); dmaB(0, i); }
        BARRIER();
#pragma unroll
        for (int i = 0; i < 4; ++i) { dmaA(1, i); dmaB(1, i); }
#pragma unroll
        for (int i = 0; i < 4; ++i) dmaB(2, i);
        u32x4 wsink = {0, 0, 0, 0}; u32x2 pv0;
        const unsigned pxf = (unsigned)((((lane & 16) ? 15 - j : j) * 16 + (lane & 15)) * 8);
        {
            waitv<20>(raw); PIN(seedv);
            tag = (((u64)rfl(seedv[1]) << 32) | rfl(seedv[0])) + 1;
            float v[8];
#pragma unroll
            for (int i = 0; i < 4; ++i) { v[2 * i] = __uint_as_float(raw[i] << 16); v[2 * i + 1] = __uint_as_float(raw[i] & 0xffff0000u); }
            float s = 0.f;
#pragma unroll
            for (int i = 0; i < 8; ++i) s += v[i];
            s = wave_sum(s);
            const float mean = s * (1.f / DM);
            float q = 0.f;
#pragma unroll
            for (int i = 0; i < 8; ++i) { const float d = v[i] - mean; q += d * d; }
            q = wave_sum(q);
            auto rb = [](float x_) { return __uint_as_float(cvtpk(x_, x_) << 16); };
            const float mb = rb(mean), rden = 1.f / rb(sqrtf(rb(rb(q * (1.f / DM)) + 1e-5f)));
            u32x4 w;
#pragma unroll
            for (int i = 0; i < 4; ++i) { const unsigned d = cvtpk(v[2 * i] - mb, v[2 * i + 1] - mb); w[i] = cvtpk(__uint_as_float(d << 16) * rden, __uint_as_float(d & 0xffff0000u) * rden); }
            st16_wt(w, mk_srd(ws + WS_XN, 2 * MiB), (unsigned)((lane >> 2) * 2048 + (lane & 3) * 16), (unsigned)((row >> 5) * 32768 + ((row >> 4) & 1) * 1024 + (row & 15) * 64));
        }
        unsigned aoff[2];
#pragma unroll
        for (int k2 = 0; k2 < 2; ++k2) { aoff[k2] = lds0 + (unsigned)((wr * 128 + (lane & 15)) * 128 + (((4 * k2 + g4) ^ ((lane >> 1) & 7)) * 16)); PIN(aoff[k2]); }
        unsigned boe = lds0 + (unsigned)(65536 + (HI ? 16384 : 0) + wc1 * 1024 + g4 * 2048 + trq * 64 + (g4 & 1) * 32 + trp * 8), boo = boe ^ 32u;
        PIN(boe); PIN(boo);
        f32x4 acc[8][4];
#pragma unroll
        for (int a = 0; a < 8; ++a)
#pragma unroll
            for (int b = 0; b < 4; ++b) { acc[a][b] = f32x4{}; PIN(acc[a][b]); }
        bf16x8 cf[4]; s16x4 kl[2][4], kh[2][4];
        auto rdA = [&](auto ac) { constexpr int ai = decltype(ac)::value;
            cf[ai & 3] = lds_rd128(lp(aoff[(ai >> 3) & 1]) + ((ai >> 4) & 1) * 32768 + (ai & 7) * 2048); };
        auto rdB = [&](int kk, auto rc) { constexpr int r = decltype(rc)::value, w = r >> 1;
            const lds_cptr p = lp((w & 1) ? boo : boe) + (kk & 1) * 8192 + (w >> 1) * 512 + (r & 1) * 256;
            if constexpr (r & 1) kh[kk & 1][w] = lds_tr(p); else kl[kk & 1][w] = lds_tr(p); };
        WAITV(13); BARRIER();
        static_for<8>([&](auto ic) { rdB(0, ic); });
        static_for<3>([&](auto ic) { rdA(ic); });
        SBAR();
        static_for<512>([&](auto nc) {
            constexpr int n = decltype(nc)::value, kt = n >> 6, g = n & 63, k2 = g >> 5, a = (g >> 2) & 7, w = g & 3, sh = HI ? 2 : 0;
            if constexpr (g == 52) {
                if constexpr (kt == 6 && !HI) { if (wid == 0) { asm volatile("s_waitcnt vmcnt(4)" : "+v"(pv0)::"memory"); poll_finish(pv0, ctl, pxf, (unsigned)WS_FX, true, tag); } }
                if constexpr (kt < 7) { boe += (kt % 3 == 2) ? (unsigned)-65536 : 32768u; boo += (kt % 3 == 2) ? (unsigned)-65536 : 32768u; }
                constexpr int wn = kt == 0 ? 5 : kt < 7 ? 4 + (kt == WKT ? 4 : 0) : 0;
                if constexpr (kt < 7) asm volatile("s_waitcnt vmcnt(%2) lgkmcnt(0)\n\ts_barrier" : "+v"(boe), "+v"(boo) : "n"(wn) : "memory");
                else asm volatile("s_waitcnt vmcnt(0) lgkmcnt(0)\n\ts_barrier" : "+v"(xf[0]), "+v"(xf[1]), "+v"(xf[2]), "+v"(xf[3]), "+v"(xf[4]), "+v"(xf[5]), "+v"(xf[6]), "+v"(xf[7])::"memory");
                if constexpr (kt == 1) { if (wid == 0) flag_set(ctl, (unsigned)(WS_FX + bx * 8), tag); }
                if constexpr (kt == WKT + 1) PIN(wsink);
                SBAR();
            }
            { const bf16x8 w_ = cat8(kl[k2][w], kh[k2][w]); constexpr int ai = n >> 2;
              if constexpr (HI) mfma16(acc[a][w], cf[ai & 3], w_); else mfma16(acc[a][w], w_, cf[ai & 3]); }
            if constexpr (w == 0 && (n >> 2) + 3 < 128) rdA(IC<(n >> 2) + 3>{});
            if constexpr (k2 == 0 && w == 2) rdB(1, IC<a>{});
            if constexpr (g > 52 && w != 0 && g != 63) { constexpr int r = (g - 53) - (g - 52) / 4;
                if constexpr (kt < 7) rdB(0, IC<r>{}); else if constexpr (r < PD) rdW(IC<r>{}); }
            if constexpr (kt >= 1 && kt <= 5 && g < 48 && g % 12 == 3 + sh) dmaB(kt + 2, g / 12);
            if constexpr (kt <= 5 && g >= 52 && g % 3 == (HI ? 0 : 1)) dmaA(kt + 2, (g - 52) / 3);
            if constexpr (kt == WKT && g == 7 + sh) warm<1>(wsink, A.wq, DM * 4096 * 2, 2 * row, vo16);
            if constexpr (kt == WKT && g == 19 + sh) warm<1>(wsink, A.wq, DM * 4096 * 2, 2 * row + 1, vo16);
            if constexpr (kt == WKT && g == 31 + sh) warm<1>(wsink, A.wg, DM * FF * 2, row, vo16);
            if constexpr (kt == WKT && g == 43 + sh) warm<1>(wsink, A.wu, DM * FF * 2, row, vo16);
            if constexpr (kt == 5 && g == 63) {
                if constexpr (!HI) { if (wid == 0) poll_issue(pv0, ctl, pxf, (unsigned)WS_FX); }
#pragma unroll
                for (int i = 0; i < 3; ++i) { const int q = (3 * w4 + i) * 64 + lane, r = q / 6, c = q - 6 * r; voF[i] = (unsigned)((r ^ ((r >> 1) & 4)) * (FF * 2) + c * 16); PIN(voF[i]); }
                bWe = lds0 + L_QA + (unsigned)(g4 * 2048 + trq * 64 + (g4 & 1) * 32 + trp * 8); bWo = bWe ^ 32u;
                bF0 = lds0 + L_FA + (unsigned)(g4 * 768 + (trq + 4 * (g4 & 1)) * 96 + trp * 8);
                bF1 = lds0 + L_FA + (unsigned)(g4 * 768 + (trq + 4 - 4 * (g4 & 1)) * 96 + trp * 8);
                PIN(bWe); PIN(bWo); PIN(bF0); PIN(bF1);
            }
            if constexpr (kt == 6 && g < 48 && g % 12 == 3 + sh) dmaW(0, IC<g / 12>{});
            if constexpr (kt == 6 && g >= 52 && g % 3 == (HI ? 0 : 1)) ldX(0, IC<(g - 52) / 3>{});
            if constexpr (kt == 6 && g >= 53 && g < 62 && g % 3 == 2) dmaW(0, IC<4 + (g - 53) / 3>{});
            if constexpr (kt == 7 && g < 16 && (g & 3) == 1 + sh) ldX(0, IC<4 + g / 4>{});
            if constexpr (kt == 7 && (g == 56 || g == 60)) dmaW(1, IC<(g - 56) / 4>{});
            SBAR();
        });
        MFMA_DRAIN(); SBAR();
#pragma unroll
        for (int i = 0; i < 16; ++i) {
            kvp[i] = HI ? frag16(acc[i >> 1][2 * (i & 1)], acc[i >> 1][2 * (i & 1) + 1]) : frag16(acc[2 * (i >> 2)][i & 3], acc[2 * (i >> 2) + 1][i & 3]);
            PIN(kvp[i]);
        }
    }
    AttSt s;
    {
        const int wr = wid & 1, wc1 = (wid >> 1) & 1;
        const srd_t srdO = mk_srd(ws + WS_KV + (size_t)h * 32 * 32768 + (HI ? 16384 : 0), 32 * 32768);
        auto stKV = [&](auto ic) {
            constexpr int i = decltype(ic)::value;
            const int u = j * 8 + wr * 4 + (i >> 2), fi = HI ? (((i >> 1) & 1) * 4 + 2 * wc1 + (i & 1)) : (4 * wc1 + (i & 3));
            st16_wt(kvp[i], srdO, vo16, (unsigned)((u >> 1) * 32768 + (u & 1) * 8192 + fi * 1024));
        };
        const int smode = j < 2 ? j : 2;
        const unsigned pkv = (unsigned)(WS_FKV + (h * 64 + j * 8 + wid) * 8);
        f32x4 acc[14][2];
#pragma unroll
        for (int d = 0; d < 14; ++d) { acc[d][0] = f32x4{}; acc[d][1] = f32x4{}; PIN(acc[d][0]); PIN(acc[d][1]); }
        asm volatile("s_nop 1"); SBAR();
        u32x2 pv;
        static_for<224>([&](auto fc) {
            constexpr int f = decltype(fc)::value, c = f / 56, r = f % 56, k = r / 14, b = r % 14, k7 = r / 7, b7 = r % 7, sh = HI ? 1 : 0;
            if constexpr (b == 0 && c > 0) {
                constexpr int wn = c < 3 ? (k == 0 ? 8 : k == 1 ? 12 : k == 2 ? 13 : 11) : (k == 0 ? 8 : k == 1 ? 10 : k == 2 ? 8 : 4);
                asm volatile("s_waitcnt vmcnt(%2)" : "+v"(xf[2 * k]), "+v"(xf[2 * k + 1]) : "n"(wn) : "memory"); SBAR();
            }
            if constexpr (r == 56 - PD && c < 3) {
                constexpr unsigned dq = (c & 1) ? (unsigned)(L_QA - L_QB) : (unsigned)(L_QB - L_QA), df = (c & 1) ? (unsigned)(L_FA - L_FB) : (unsigned)(L_FB - L_FA);
                bWe += dq; bWo += dq; bF0 += df; bF1 += df;
                asm volatile("s_waitcnt vmcnt(4) lgkmcnt(0)\n\ts_barrier" : "+v"(bWe), "+v"(bWo), "+v"(bF0), "+v"(bF1)::"memory");
                SBAR();
            }
            if constexpr (HI && (r == 0 || (r == 56 - PD && c < 3))) __builtin_amdgcn_s_setprio(1);
            if constexpr (HI && r == 24) __builtin_amdgcn_s_setprio(0);
            { const bf16x8 w_ = cat8(wl[f % (PD + 1)], wh[f % (PD + 1)]);
              if (b < 12 || nu == 3) { mfma16(acc[b][0], w_, xf[2 * k]); mfma16(acc[b][1], w_, xf[2 * k + 1]); } }
            if constexpr (f + PD < 224) rdW(IC<f + PD>{});
            {
                constexpr int p = r == 53 ? 0 : r == 55 ? 1 : r == 2 + sh ? 2 : r == 4 + sh ? 3 : r == 9 + sh ? 4 : r == 11 + sh ? 5 : r == 16 + sh ? 6 : -1;
                constexpr int cw = p < 2 ? c + 2 : c + 1;
                if constexpr (p >= 0 && cw < 4) dmaW(cw, IC<p>{});
                if constexpr (p >= 0 && p < 6 && cw == 4) dma16s(lds0 + wid * 6144 + p * 1024, vo16, srdKV, (unsigned)(wid * 6144 + p * 1024));
            }
            if constexpr (c == 0 && b7 == (HI ? 4 : 1)) { if (smode == 0) stKV(IC<2 * k7>{}); }
            if constexpr (b7 == (HI ? 6 : 3)) {
                if constexpr (c == 0) { if (smode == 0) stKV(IC<2 * k7 + 1>{}); }
                if constexpr (c < 2) { if (smode == 1) stKV(IC<8 * (c & 1) + k7>{}); }
                if constexpr ((k7 & 1) == (HI ? 1 : 0)) { if (smode == 2) stKV(IC<4 * c + (k7 >> 1)>{}); }
            }
            if constexpr (b == 13) {
                if constexpr (c < 3) { ldX(c + 1, IC<2 * k>{}); ldX(c + 1, IC<2 * k + 1>{}); }
                if constexpr ((c == 1 || c == 2) && k == 1) { if (smode == c - 1) { WAITV(11); flag_set(ctl, pkv, tag); } }
                if constexpr (!HI && c == 2 && k == 0) { if (wid == 0) poll_issue(pv, ctl, vo16 >> 1, (unsigned)(WS_FKV + h * 512)); }
                if constexpr (!HI && c == 2 && k == 2) { if (wid == 0) { waitv<5>(pv); poll_finish(pv, ctl, vo16 >> 1, (unsigned)(WS_FKV + h * 512), vo16 < 128, tag); } }
                if constexpr (!HI && c == 3 && k == 1) { if (wid == 0) poll_issue(pv, ctl, vo16 >> 1, (unsigned)(WS_FKV + h * 512)); }
                if constexpr (!HI && c == 3 && k == 2) { if (wid == 0) { waitv<0>(pv); poll_finish(pv, ctl, vo16 >> 1, (unsigned)(WS_FKV + h * 512), vo16 < 256, tag); } }
            }
            SBAR();
        });
        MFMA_DRAIN(); SBAR();
        const srd_t srdH = mk_srd(ws + WS_CH + (size_t)(qg >> 1) * TSTR + (256 + u0) * 2048 + (qg & 1) * 1024, (unsigned)nu * 2048 - (qg & 1) * 1024);
#pragma unroll
        for (int v = 0; v < 3; ++v) {
            f32x4 hv[2];
#pragma unroll
            for (int r = 0; r < 8; ++r) {
                const float g = acc[8 + 2 * v][r >> 2][r & 3];
                const float z = g * (1.5957691216f + 0.0713548163f * g * g);
                const float e = __builtin_amdgcn_exp2f(-1.4426950409f * z);
                hv[r >> 2][r & 3] = g * __builtin_amdgcn_rcpf(1.f + e) * acc[9 + 2 * v][r >> 2][r & 3];
            }
            st16_wt(frag16(hv[0], hv[1]), srdH, vo16, (unsigned)v * 2048);
        }
        {
            const srd_t so = mk_srd(A.wo, 4096 * DM * 2), sd = mk_srd(A.wd, FF * DM * 2); const unsigned gw = (unsigned)(bx * 8 + wid);
            dma16(lds0 + L_QA + wid * 1024, vo16, so, gw * 2048u); dma16(lds0 + L_QA + wid * 1024, vo16, so, gw * 2048u + 1024u); dma16(lds0 + L_QA + wid * 1024, vo16, sd, gw * 1024u);
        }
        SBAR();
#pragma unroll
        for (int d = 0; d < 8; ++d) { s.qf[d] = __builtin_bit_cast(bf16x8, frag16(acc[d][0] * QSCALE, acc[d][1] * QSCALE)); PIN(s.qf[d]); }
        asm volatile("s_waitcnt vmcnt(3) lgkmcnt(0)\n\ts_barrier" ::: "memory");
        dma16x4(lds0 + 49152 + wid * 4096, vo16, srdKV, (unsigned)(49152 + wid * 4096));
        flag_set(ctl, (unsigned)(WS_FH + (qg * 32 + h) * 8), tag);
        if (smode == 2) flag_set(ctl, pkv, tag);
    }
    bool cpub = false;
    const int rt = (xcd >> 1) * 8 + (bi >> 2), ct = (xcd & 1) * 4 + (bi & 3);
    const int hp = (h & 7) * 4 + (h >> 3);
    const unsigned pcf = (unsigned)(WS_FC + (qg * 32 + hp) * 8);
    const int s0 = HI ? 172 + 21 * w4 : 43 * w4, sn = HI ? 21 : 43;
    const int f0 = w4 * 22 - (w4 > 2 ? 1 : 0), fn = w4 < 2 ? 22 : 21;
    const srd_t srdH = mk_srd(ws + WS_CH + (size_t)rt * TSTR + (256 + f0) * 2048, (unsigned)fn * 2048u), srdD = mk_srd(ws + WS_WF + (size_t)ct * TSTR + (256 + f0) * 2048, (unsigned)fn * 2048u);
    const srd_t srdC = mk_srd(ws + WS_CH + (size_t)rt * TSTR + s0 * 2048, (unsigned)sn * 2048u), srdO = mk_srd(ws + WS_WF + (size_t)ct * TSTR + s0 * 2048, (unsigned)sn * 2048u);
    constexpr int NF = HI ? 22 : 0, RD = HI ? RDH : 8;
    bf16x8 bf[RD][2], wf[RD][2];
    auto isW = [&](auto nc) { constexpr int n = decltype(nc)::value;
        bufld16<0>(wf[n % RD][0], vo16, n < NF ? srdD : srdO, (unsigned)(n < NF ? n : n - NF) * 2048u); bufld16<1024>(wf[n % RD][1], vo16, n < NF ? srdD : srdO, (unsigned)(n < NF ? n : n - NF) * 2048u); };
    auto isB = [&](auto nc) { constexpr int n = decltype(nc)::value;
        bufld16<0>(bf[n % RD][0], vo16, n < NF ? srdH : srdC, (unsigned)(n < NF ? n : n - NF) * 2048u); bufld16<1024>(bf[n % RD][1], vo16, n < NF ? srdH : srdC, (unsigned)(n < NF ? n : n - NF) * 2048u); };
    const unsigned pcc = (unsigned)(WS_FC + rt * 512), phh = (unsigned)(WS_FH + rt * 512);
#define NEEDC (((vo16 >> 4) & 31u) - (unsigned)(s0 >> 3) <= (unsigned)(((s0 + sn - 1) >> 3) - (s0 >> 3)))
    {
        const int lane = lane_id(), r32 = lane & 31, hi = lane >> 5;
        const int tl = qg >> 1;
        const int row = qg * 32 + r32;
        const unsigned dvo = (unsigned)(lane * 16 + w4 * 4096);
        const unsigned m0b = lds0 + w4 * 4096;
        f32x16 Sa, Sb; u32x4 Pa[2], Pb[2];
        Pb[0] = u32x4{0, 0, 0, 0}; Pb[1] = u32x4{0, 0, 0, 0}; Pa[0] = Pb[0]; Pa[1] = Pb[0];
        unsigned P = lds0 + vo16, Q = lds0 + 65536 + vo16;
        PIN(P); PIN(Q);
        {
#pragma unroll
            for (int i = 0; i < 8; ++i) s.fr[i] = lds_rd128(lp(P) + i * 1024);
            SBAR();
            f32x16 z = f32x16{};
            static_for<8>([&](auto ic) { constexpr int i = decltype(ic)::value; z = MFMA(s.fr[i], s.qf[i], z);
                if constexpr (i == 1 || i == 3 || i == 5) { s.fr[i] = lds_rd128(lp(P) + 8192 + i * 1024); s.fr[i - 1] = lds_rd128(lp(P) + 8192 + (i - 1) * 1024); } SBAR(); });
            Sa = z;
            if (tl == 0) cmask(Sa, row - 4 * hi);
            float ma = MX3(Sa[0], Sa[1], Sa[2]), mb = MX3(Sa[3], Sa[4], Sa[5]);
            ma = MX3(ma, Sa[6], Sa[7]); mb = MX3(mb, Sa[8], Sa[9]); ma = MX3(ma, Sa[10], Sa[11]); mb = MX3(mb, Sa[12], Sa[13]); ma = MX3(ma, Sa[14], Sa[15]);
            ma = __builtin_fmaxf(ma, mb);
            auto rr = __builtin_amdgcn_permlane32_swap(__float_as_uint(ma), __float_as_uint(ma), false, false);
            const float rm = __builtin_fmaxf(__uint_as_float(rr[0]), __uint_as_float(rr[1]));
            s.m = rm; s.l = 0.f;
#pragma unroll
            for (int r = 0; r < 16; ++r) { Sa[r] -= rm; s.negm[r] = -rm; }
#pragma unroll
            for (int d = 0; d < 4; ++d) s.o[d] = f32x16{};
            PIN(s.negm);
        }
#define STEP_END() do { if constexpr (HI) WAITV(8); BARRIER(); } while (0)
#define PAIR(MID, END) do { \
                half_step<HI, false, true>(s, Sb, Sa, Pa, Pb, lp(P) + 8192, lp(Q) + 57344, lp(P) + 32768, lp(P) + 16384, 0, sQ + 32768, dvo, srdKV, sk); \
                half_step<HI, false, true>(s, Sa, Sb, Pb, Pa, lp(P) + 32768, lp(P) + 16384, lp(P) + 40960, lp(P) + 24576, 0, sQ + 16384, dvo, srdKV, sk - 16384u); \
                MID; STEP_END(); \
                half_step<HI, false, true>(s, Sb, Sa, Pa, Pb, lp(P) + 40960, lp(P) + 24576, lp(Q), lp(P) + 49152, 0, sP, dvo, srdKV, sk + 32768u); \
                half_step<HI, false, true>(s, Sa, Sb, Pb, Pa, lp(Q), lp(P) + 49152, lp(Q) + 8192, lp(P) + 57344, 0, sQ + 49152, dvo, srdKV, sk + 16384u); \
                P ^= 65536u; Q ^= 65536u; sP ^= 65536u; sQ ^= 65536u; sk += 65536u; \
                if constexpr (HI) WAITV(8); \
                END; asm volatile("s_barrier" : "+v"(P), "+v"(Q)::"memory"); } while (0)
        int t = 0;
        const int npair = (tl - 1) >> 1;
        if (!HI || npair > 0) {
            unsigned sP = m0b, sQ = m0b + 65536, sk = 3 * 32768u;
            int it = 0;
            if constexpr (!HI) {
                u32x2 pv;
                PAIR(WAITV(0); if (wid == 0) poll_issue(pv, ctl, vo16 >> 1, (unsigned)(WS_FKV + h * 512)),
                     if (wid == 0) { waitv<0>(pv); poll_finish(pv, ctl, vo16 >> 1, (unsigned)(WS_FKV + h * 512), (vo16 >> 7) <= (unsigned)((15 - j) >> 1), tag); });
                it = 1;
            }
            int left = rfl(npair - it);
#pragma unroll 1
            for (; left > 0; --left) { PAIR((void)0, (void)0); asm volatile("" : "+s"(left)); }
            t = 2 * npair;
        }
#undef PAIR
        const unsigned B0 = lds0 + vo16;
#pragma unroll 1
        for (; t < tl; ++t) {
            const lds_cptr c = lp(B0 + (t & 3) * 32768), n = lp(B0 + ((t + 1) & 3) * 32768), p = lp(B0 + ((t - 1) & 3) * 32768);
            half_step<HI, false, true>(s, Sb, Sa, Pa, Pb, c + 8192, p + 24576, n, c + 16384, 0, rfl(m0b + ((t + 3) & 3) * 32768), dvo, srdKV, (unsigned)(t + 3) * 32768u);
            half_step<HI, true, true>(s, Sa, Sb, Pb, Pa, n, c + 16384, n + 8192, c + 24576, row - (64 * t + 64) - 4 * hi, rfl(m0b + ((t + 2) & 3) * 32768 + 16384), dvo, srdKV, (unsigned)(t + 2) * 32768u + 16384u);
            STEP_END();
        }
        {
            const lds_cptr c = lp(B0 + (t & 3) * 32768), p = lp(B0 + ((t - 1) & 3) * 32768);
            half_step<HI, true, true>(s, Sb, Sa, Pa, Pb, c + 8192, p + 24576, c, c + 16384, row - (64 * t + 32) - 4 * hi, rfl(m0b + ((t + 3) & 3) * 32768), dvo, srdKV, (unsigned)(t + 3) * 32768u);
            half_step<HI, false, false>(s, Sa, Sb, Pb, Pa, c, c + 16384, c + 24576, c, 0, rfl(m0b + ((t + 2) & 3) * 32768 + 16384), dvo, srdKV, (unsigned)(t + 2) * 32768u + 16384u);
            s.fr[7] = lds_rd128(c + 24576 + 7 * 1024); s.fr[6] = lds_rd128(c + 24576 + 6 * 1024);
#pragma unroll
            for (int i = 0; i < 8; ++i) s.o[i & 3] = MFMA(s.fr[i], __builtin_bit_cast(bf16x8, Pb[i >> 2]), s.o[i & 3]);
            asm volatile("s_waitcnt lgkmcnt(0)" ::: "memory");
            STEP_END();
            ++t;
        }
#undef STEP_END
        {
            auto rr = __builtin_amdgcn_permlane32_swap(__float_as_uint(s.l), __float_as_uint(s.l), false, false);
            const float inv = __builtin_amdgcn_rcpf(__uint_as_float(rr[0]) + __uint_as_float(rr[1]));
            const srd_t srdCo = mk_srd(ws + WS_CH + (size_t)(qg >> 1) * TSTR + hp * 16384 + (qg & 1) * 1024, 16384);
#pragma unroll
            for (int d = 0; d < 4; ++d)
#pragma unroll
                for (int q = 0; q < 2; ++q) st16_wt(pack8(s.o[d], q, inv), srdCo, vo16, (unsigned)(d * 2 + q) * 2048u);
        }
        if constexpr (HI) {
#define KVDMA() do { dma16x4(rfl(m0b + ((t + 3) & 3) * 32768), dvo, srdKV, (unsigned)(t + 3) * 32768u); \
                     dma16x4(rfl(m0b + ((t + 2) & 3) * 32768 + 16384), dvo, srdKV, (unsigned)(t + 2) * 32768u + 16384u); } while (0)
            if (t + 2 < NT) {
                KVDMA(); WAITV(16); BARRIER(); ++t;
                if (t + 2 < NT) { KVDMA(); WAITV(8); flag_set(ctl, pcf, tag); cpub = true; BARRIER(); ++t; }
                if (j <= 2) {
                    const unsigned voT = (unsigned)(((lane >> 2) & 7) * 1024 + (lane >> 5) * 64 + (lane & 3) * 16);
                    const lds_cptr tp = lp(lds0 + 131072 + w4 * 8192 + (4 * hi + ((lane & 15) >> 2)) * 64 + ((lane >> 4) & 1) * 32 + (lane & 3) * 8);
                    const int un_ = (h * 3 + j) * 4 + w4, hf = un_ & 1;
                    auto tin = [&](int tks) {
                        const srd_t srdT = mk_srd((tks < 256 ? A.wo + (size_t)tks * 16 * DM : A.wd + (size_t)(tks - 256) * 16 * DM) + hf * 256, tks < 342 ? 16 * DM * 2 - hf * 512 : 0);
#pragma unroll
                        for (int i = 0; i < 8; ++i) dma16(lds0 + 131072 + w4 * 8192 + i * 1024, voT, srdT, (unsigned)((i >> 2) * 8192 + (i & 3) * 128));
                    };
                    auto tout = [&](int tks) {
                        const int tp_ = tks < 256 ? (((tks >> 3) & 7) * 4 + (tks >> 6)) * 8 + (tks & 7) : tks;
                        const srd_t srdTo = mk_srd(ws + WS_WF + (size_t)hf * 4 * TSTR + tp_ * 2048, tks < 342 ? 3 * TSTR + 2048 : 0);
#pragma unroll
                        for (int nb = 0; nb < 8; ++nb) st16_wt(__builtin_bit_cast(u32x4, cat8(lds_tr(tp + nb * 512), lds_tr(tp + 4096 + nb * 512))), srdTo, vo16, (unsigned)((nb >> 1) * TSTR + (nb & 1) * 1024));
                    };
                    const int k1 = un_ >> 1, k2 = k1 + 192;
                    KVDMA(); tin(k1); WAITV(16); BARRIER(); ++t;
                    KVDMA(); WAITV(16); BARRIER(); ++t;
                    KVDMA(); WAITV(8); BARRIER(); ++t;
                    KVDMA(); tout(k1); tin(k2); WAITV(24); BARRIER(); ++t;
                    KVDMA(); WAITV(24); BARRIER(); ++t;
                    KVDMA(); WAITV(8); flag_set(ctl, (unsigned)(WS_FW + (hf * 384 + k1) * 8), tag); BARRIER(); ++t;
                    KVDMA(); tout(k2); WAITV(16); BARRIER(); ++t;
                    KVDMA(); WAITV(16); BARRIER(); ++t;
                    KVDMA(); WAITV(8); flag_set(ctl, (unsigned)(WS_FW + (hf * 384 + k2) * 8), tag); BARRIER(); ++t;
                }
#pragma unroll 1
                for (; t + 2 < NT; ++t) { KVDMA(); WAITV(8); BARRIER(); }
            }
#undef KVDMA
            u32x2 ph; u32x4 pw[3];
            const unsigned pfw = (unsigned)(WS_FW + (ct >> 2) * 3072);
            asm volatile("buffer_load_dwordx4 %0, %3, %4, %5 offen sc1\n\tbuffer_load_dwordx4 %1, %3, %4, %5 offen offset:1024 sc1\n\tbuffer_load_dwordx4 %2, %3, %4, %5 offen offset:2048 sc1"
                         : "=&v"(pw[0]), "=&v"(pw[1]), "=&v"(pw[2]) : "v"(vo16), "s"(ctl), "s"(pfw) : "memory");
            poll_issue(ph, ctl, vo16 >> 1, phh);
            asm volatile("s_waitcnt vmcnt(0)" : "+v"(ph), "+v"(pw[0]), "+v"(pw[1]), "+v"(pw[2])::"memory");
            if (!cpub) { flag_set(ctl, pcf, tag); cpub = true; }
#pragma unroll
            for (int i = 0; i < 6; ++i) poll_finish(u32x2{pw[i >> 1][2 * (i & 1)], pw[i >> 1][2 * (i & 1) + 1]}, ctl, vo16, pfw + (i >> 1) * 1024 + (i & 1) * 8, i < 4 || vo16 < 688, tag);
            static_for<RD>([&](auto ic) { isW(ic); });
            BARRIER();
            poll_finish(ph, ctl, vo16 >> 1, phh, true, tag);
            static_for<RD>([&](auto ic) { isB(ic); });
            BARRIER();
        } else {
            u32x2 pc;
            static_for<RD>([&](auto ic) { isW(ic); });
            WAITV(16);
            flag_set(ctl, pcf, tag);
            poll_issue(pc, ctl, vo16 >> 1, pcc);
            if (t < NT) BARRIER();
            waitv<0>(pc);
            poll_finish(pc, ctl, vo16 >> 1, pcc, NEEDC, tag);
            static_for<RD>([&](auto ic) { isB(ic); });
        }
    }
    {
        const int lane = lane_id();
        f32x16 acc[2][2];
#pragma unroll
        for (int i = 0; i < 4; ++i) acc[i >> 1][i & 1] = f32x16{};
#define PUT_PARTIAL(ac) do { _Pragma("unroll") for (int i_ = 0; i_ < 16; ++i_) { const f32x16& a_ = ac[i_ >> 3][(i_ >> 2) & 1]; const int g_ = i_ & 3; \
            *(LAS f32x4*)(lds + wid * 17408 + i_ * 1088 + vo16) = f32x4{a_[4 * g_], a_[4 * g_ + 1], a_[4 * g_ + 2], a_[4 * g_ + 3]}; } } while (0)
#define GET_SUMS(dst, w0) do { _Pragma("unroll") for (int uu = 0; uu < 2; ++uu) { f32x4 sv = {0.f, 0.f, 0.f, 0.f}; \
            _Pragma("unroll") for (int w = w0; w < 8; ++w) sv += *(const LAS f32x4*)(lp(ga + (w >> 1) * 34816) + (w & 1) * 17408 + uu * 512); dst[uu] = sv; } } while (0)
        const int orow = wid * 8 + (lane >> 3), c8 = lane & 7;
        const unsigned ga = lds0 + (unsigned)(((c8 >> 2) * 8 + (wid >> 2) * 4 + (c8 & 3)) * 1088 + (orow & 31) * 16);
        const unsigned ooff = (unsigned)(((rt * 64 + orow) * DM + ct * 64 + c8 * 8) * 2);
        u32x2 pc; bf16x8 xr;
        const unsigned vo8 = vo16 >> 1;
        static_for<43>([&](auto nc) {
            constexpr int n = decltype(nc)::value, sl = n % RD;
            constexpr int w0 = 2 * (RD - 1 - n) + 4 * n;
            constexpr int wn = (n < RD && w0 < 4 * (RD - 1)) ? w0 : n + RD <= 43 ? 4 * (RD - 1) : 4 * (42 - n);
            wait4<wn>(bf[sl][0], bf[sl][1], wf[sl][0], wf[sl][1]);
            acc[0][0] = MFMA(wf[sl][0], bf[sl][0], acc[0][0]); acc[1][0] = MFMA(wf[sl][1], bf[sl][0], acc[1][0]);
            acc[0][1] = MFMA(wf[sl][0], bf[sl][1], acc[0][1]); acc[1][1] = MFMA(wf[sl][1], bf[sl][1], acc[1][1]);
            PIN(acc[0][0]); PIN(acc[0][1]); PIN(acc[1][0]); PIN(acc[1][1]); SBAR();
            if constexpr (HI && n == NF - RD - 4) poll_issue(pc, ctl, vo8, pcc);
            if constexpr (HI && n == NF - RD) { waitv<16>(pc); poll_finish(pc, ctl, vo8, pcc, NEEDC, tag); }
            if constexpr (n + RD < 43) { isW(IC<n + RD>{}); isB(IC<n + RD>{}); }
            if constexpr (n == 36) bufld16<0>(xr, ooff, mk_srd(A.x, 2048 * DM * 2), 0u);
            if constexpr (HI && n == NF - 1) {
                PUT_PARTIAL(acc);
#pragma unroll
                for (int i = 0; i < 4; ++i) acc[i >> 1][i & 1] = f32x16{};
            }
            SBAR();
        });
        waitv<0>(xr);
        f32x4 red[2][2];
        if constexpr (!HI) PUT_PARTIAL(acc);
        asm volatile("s_waitcnt lgkmcnt(0)\n\ts_barrier" ::: "memory");
        GET_SUMS(red[0], 4);
        asm volatile("s_waitcnt lgkmcnt(0)\n\ts_barrier" : "+v"(red[0][0]), "+v"(red[0][1])::"memory");
        if constexpr (HI) PUT_PARTIAL(acc);
        asm volatile("s_waitcnt lgkmcnt(0)\n\ts_barrier" ::: "memory");
        GET_SUMS(red[1], 0);
#undef PUT_PARTIAL
#undef GET_SUMS
        {
            const u32x4 xw = __builtin_bit_cast(u32x4, xr);
            u32x4 ow;
#pragma unroll
            for (int e = 0; e < 4; ++e) {
                const float f0 = red[0][e >> 1][2 * (e & 1)], f1 = red[0][e >> 1][2 * (e & 1) + 1];
                const float g0 = f0 * __builtin_amdgcn_rcpf(1.f + __builtin_amdgcn_exp2f(-1.4426950409f * f0)), g1 = f1 * __builtin_amdgcn_rcpf(1.f + __builtin_amdgcn_exp2f(-1.4426950409f * f1));
                ow[e] = cvtpk(__uint_as_float(xw[e] << 16) + red[1][e >> 1][2 * (e & 1)] * g0, __uint_as_float(xw[e] & 0xffff0000u) + red[1][e >> 1][2 * (e & 1) + 1] * g1);
            }
            *(u32x4*)((char*)A.out + ooff) = ow;
        }
        if (rt == 31 && ct == 7 && wid == 0) flag_set(ctl, (unsigned)WS_SEED, tag);
    }
}

__global__ __launch_bounds__(512, 2) void k_fused(Args A) {
    extern __shared__ __attribute__((aligned(16))) char lds_raw[];
    const int wid = rfl(threadIdx.x >> 6);
    if (wid >= 4) run<true>(A, (lds_ptr)lds_raw, wid);
    else run<false>(A, (lds_ptr)lds_raw, wid);
}

extern "C" void kernel_launch(void* const* d_in, const int* in_sizes, int n_in, void* d_out, int out_size, void* d_ws, size_t ws_size,
                              hipStream_t stream) {
    Args A;
    A.x = (const u16*)d_in[0]; A.ckv = (const u16*)d_in[1]; A.wq = (const u16*)d_in[2]; A.wuk = (const u16*)d_in[3]; A.wuv = (const u16*)d_in[4];
    A.wo = (const u16*)d_in[5]; A.wg = (const u16*)d_in[7]; A.wu = (const u16*)d_in[8]; A.wd = (const u16*)d_in[9];
    A.out = (u16*)d_out; A.ws = (char*)d_ws;
    (void)hipFuncSetAttribute((const void*)k_fused, hipFuncAttributeMaxDynamicSharedMemorySize, LDS_BYTES);
    k_fused<<<256, 512, LDS_BYTES, stream>>>(A);
}
```
